```python
import torch

def _softmax(x, axis=-1):
    return torch.softmax(x, dim=axis)
def _gelu(x):
    return torch.nn.functional.gelu(x, approximate="tanh")
def _silu(x):
    return torch.nn.functional.silu(x)
def _sigmoid(x):
    return torch.sigmoid(x)
def _layernorm(x, axis=-1, eps=1e-5):
    m = x.mean(dim=axis, keepdim=True)
    v = x.var(dim=axis, keepdim=True, unbiased=False)
    return (x - m) / torch.sqrt(v + eps)
def _scatter_add(tgt, idx, src, axis=0):
    return tgt.clone().index_add_(axis, idx, src)
def _segment_sum(src, seg, n):
    out = torch.zeros((n,) + src.shape[1:], dtype=src.dtype)
    return out.index_add_(0, seg, src)
def _pad(x, pw):
    flat = tuple(v for pair in reversed(pw) for v in pair)
    return torch.nn.functional.pad(x, flat)

kernel_name = 'sequential_preLN__spectral_gfnet__glu_gated_geglu__prompt'

def setup_inputs():

    _w = (lambda a: (lambda t: t.to(torch.bfloat16) if t.is_floating_point() else t)(torch.as_tensor(a)))
    def _A_setup():
        B, S, D = 1, 2048, 1024
        Df = D // 2 + 1
        return {
            "x": _w((lambda *s: torch.randn(*s, dtype=torch.bfloat16) * 0.05)(B, S, D)),
            "w_r": _w((lambda *s: torch.randn(*s, dtype=torch.bfloat16) * 0.4)(S, Df) * 0.02),
            "w_i": _w((lambda *s: torch.randn(*s, dtype=torch.bfloat16) * 0.4)(S, Df) * 0.02),
        }
    _A_in = _A_setup()

    def _B_setup():
        B, S, Dm, Df = 1, 2048, 1024, 3584
        return {
            "x": _w((lambda *s: torch.randn(*s, dtype=torch.bfloat16) * 0.05)(B, S, Dm)),
            "w_gate": _w((lambda *s: torch.randn(*s, dtype=torch.bfloat16) * 0.4)(Dm, Df) * (Dm ** -0.5)),
            "w_up": _w((lambda *s: torch.randn(*s, dtype=torch.bfloat16) * 0.4)(Dm, Df) * (Dm ** -0.5)),
            "w_down": _w((lambda *s: torch.randn(*s, dtype=torch.bfloat16) * 0.4)(Df, Dm) * (Df ** -0.5)),
        }
    _B_in = _B_setup()

    return {
        "A_x": _A_in["x"],
        "A_w_r": _A_in["w_r"],
        "A_w_i": _A_in["w_i"],
        "B_x": _B_in["x"],
        "B_w_gate": _B_in["w_gate"],
        "B_w_up": _B_in["w_up"],
        "B_w_down": _B_in["w_down"],
    }

def reference(A_x, A_w_r, A_w_i, B_x, B_w_gate, B_w_up, B_w_down):
    def _A_ref(x, w_r, w_i):
        B, S, D = x.shape
        X = (lambda x, axes=(-2,-1): torch.fft.rfft2(x.float(), dim=axes))(x, axes=(-2, -1))
        W = w_r + 1j * w_i
        X = X * W
        y = (lambda x, s, axes=(-2,-1): torch.fft.irfft2(x, s=s, dim=axes).to(torch.bfloat16))(X, (S, D), axes=(-2, -1))
        return y

    def _B_ref(x, w_gate, w_up, w_down):
        g = torch.einsum("bsd,df->bsf", x, w_gate)
        u = torch.einsum("bsd,df->bsf", x, w_up)
        h = _gelu(g) * u
        return torch.einsum("bsf,fd->bsd", h, w_down)

    x = A_x
    h = x + _A_ref(_layernorm(x, axis=-1), A_w_r, A_w_i)
    h2 = h + _B_ref(_layernorm(h, axis=-1), B_w_gate, B_w_up, B_w_down)
    return h2
```

```cpp
#include <hip/hip_runtime.h>
#include <hip/hip_bf16.h>
#include <stdint.h>

typedef float f32x2 __attribute__((ext_vector_type(2)));
typedef float f32x4 __attribute__((ext_vector_type(4)));
typedef short bf16x8 __attribute__((ext_vector_type(8)));
typedef unsigned u32x4 __attribute__((ext_vector_type(4)));
typedef unsigned u32x2 __attribute__((ext_vector_type(2)));
typedef unsigned short u16;

#define S_ 2048
#define D_ 1024
#define DF_ 3584
#define NF_ 513

__device__ const float TWT[4096] = {
1.000000000e+00f,-0.000000000e+00f,9.999952912e-01f,-3.067956772e-03f,9.999811649e-01f,-6.135884672e-03f,9.999576211e-01f,-9.203754365e-03f,9.999247193e-01f,-1.227153838e-02f,9.998823404e-01f,-1.533920597e-02f,9.998306036e-01f,-1.840673015e-02f,9.997693896e-01f,-2.147408016e-02f,
9.996988177e-01f,-2.454122901e-02f,9.996188283e-01f,-2.760814503e-02f,9.995294213e-01f,-3.067480400e-02f,9.994305968e-01f,-3.374117240e-02f,9.993223548e-01f,-3.680722415e-02f,9.992047548e-01f,-3.987292573e-02f,9.990777373e-01f,-4.293825850e-02f,9.989413023e-01f,-4.600318149e-02f,
9.987954497e-01f,-4.906767607e-02f,9.986402392e-01f,-5.213170499e-02f,9.984755516e-01f,-5.519524589e-02f,9.983015656e-01f,-5.825826526e-02f,9.981181026e-01f,-6.132073700e-02f,9.979252815e-01f,-6.438262761e-02f,9.977230430e-01f,-6.744392216e-02f,9.975114465e-01f,-7.050457597e-02f,
9.972904325e-01f,-7.356456667e-02f,9.970600605e-01f,-7.662386447e-02f,9.968202710e-01f,-7.968243957e-02f,9.965711236e-01f,-8.274026215e-02f,9.963126183e-01f,-8.579730988e-02f,9.960446954e-01f,-8.885355294e-02f,9.957674146e-01f,-9.190895408e-02f,9.954807758e-01f,-9.496349841e-02f,
9.951847196e-01f,-9.801714122e-02f,9.948793054e-01f,-1.010698602e-01f,9.945645928e-01f,-1.041216329e-01f,9.942404628e-01f,-1.071724221e-01f,9.939069748e-01f,-1.102222055e-01f,9.935641289e-01f,-1.132709533e-01f,9.932119250e-01f,-1.163186282e-01f,9.928504229e-01f,-1.193652153e-01f,
9.924795628e-01f,-1.224106774e-01f,9.920992851e-01f,-1.254549772e-01f,9.917097688e-01f,-1.284981072e-01f,9.913108349e-01f,-1.315400302e-01f,9.909026623e-01f,-1.345807016e-01f,9.904850721e-01f,-1.376201212e-01f,9.900581837e-01f,-1.406582445e-01f,9.896219969e-01f,-1.436950266e-01f,
9.891765118e-01f,-1.467304677e-01f,9.887216687e-01f,-1.497645378e-01f,9.882575870e-01f,-1.527971923e-01f,9.877841473e-01f,-1.558284014e-01f,9.873014092e-01f,-1.588581502e-01f,9.868093729e-01f,-1.618863940e-01f,9.863080978e-01f,-1.649131179e-01f,9.857975245e-01f,-1.679382920e-01f,
9.852776527e-01f,-1.709618866e-01f,9.847484827e-01f,-1.739838719e-01f,9.842100739e-01f,-1.770042181e-01f,9.836624265e-01f,-1.800228953e-01f,9.831054807e-01f,-1.830398887e-01f,9.825392962e-01f,-1.860551536e-01f,9.819638729e-01f,-1.890686601e-01f,9.813792109e-01f,-1.920803934e-01f,
9.807852507e-01f,-1.950903237e-01f,9.801821113e-01f,-1.980984062e-01f,9.795697927e-01f,-2.011046410e-01f,9.789481759e-01f,-2.041089684e-01f,9.783173800e-01f,-2.071113735e-01f,9.776773453e-01f,-2.101118416e-01f,9.770281315e-01f,-2.131103128e-01f,9.763697386e-01f,-2.161068022e-01f,
9.757021070e-01f,-2.191012353e-01f,9.750253558e-01f,-2.220936269e-01f,9.743393660e-01f,-2.250839174e-01f,9.736442566e-01f,-2.280720770e-01f,9.729399681e-01f,-2.310581058e-01f,9.722265005e-01f,-2.340419590e-01f,9.715039134e-01f,-2.370236069e-01f,9.707721472e-01f,-2.400030196e-01f,
9.700312614e-01f,-2.429801822e-01f,9.692812562e-01f,-2.459550500e-01f,9.685220718e-01f,-2.489276081e-01f,9.677538276e-01f,-2.518978119e-01f,9.669764638e-01f,-2.548656464e-01f,9.661899805e-01f,-2.578310966e-01f,9.653944373e-01f,-2.607941031e-01f,9.645897746e-01f,-2.637546659e-01f,
9.637760520e-01f,-2.667127550e-01f,9.629532695e-01f,-2.696683109e-01f,9.621214271e-01f,-2.726213634e-01f,9.612804651e-01f,-2.755718231e-01f,9.604305029e-01f,-2.785196900e-01f,9.595715404e-01f,-2.814649343e-01f,9.587034583e-01f,-2.844075263e-01f,9.578264356e-01f,-2.873474658e-01f,
9.569403529e-01f,-2.902846634e-01f,9.560452700e-01f,-2.932191491e-01f,9.551411867e-01f,-2.961508930e-01f,9.542281032e-01f,-2.990798354e-01f,9.533060193e-01f,-3.020059466e-01f,9.523749948e-01f,-3.049292266e-01f,9.514350295e-01f,-3.078496456e-01f,9.504860640e-01f,-3.107671440e-01f,
9.495281577e-01f,-3.136817515e-01f,9.485613704e-01f,-3.165933788e-01f,9.475855827e-01f,-3.195020258e-01f,9.466009140e-01f,-3.224076927e-01f,9.456073046e-01f,-3.253102899e-01f,9.446048141e-01f,-3.282098472e-01f,9.435934424e-01f,-3.311063051e-01f,9.425731897e-01f,-3.339996636e-01f,
9.415440559e-01f,-3.368898630e-01f,9.405060410e-01f,-3.397768736e-01f,9.394592047e-01f,-3.426607251e-01f,9.384035468e-01f,-3.455413282e-01f,9.373390079e-01f,-3.484186828e-01f,9.362656474e-01f,-3.512927592e-01f,9.351835251e-01f,-3.541635275e-01f,9.340925217e-01f,-3.570309579e-01f,
9.329928160e-01f,-3.598950505e-01f,9.318842888e-01f,-3.627557158e-01f,9.307669401e-01f,-3.656129837e-01f,9.296408892e-01f,-3.684668243e-01f,9.285060763e-01f,-3.713172078e-01f,9.273625016e-01f,-3.741640747e-01f,9.262102246e-01f,-3.770074248e-01f,9.250492454e-01f,-3.798471987e-01f,
9.238795042e-01f,-3.826834261e-01f,9.227011204e-01f,-3.855160475e-01f,9.215140343e-01f,-3.883450329e-01f,9.203183055e-01f,-3.911703825e-01f,9.191138744e-01f,-3.939920366e-01f,9.179008007e-01f,-3.968099952e-01f,9.166790843e-01f,-3.996241987e-01f,9.154487252e-01f,-4.024346471e-01f,
9.142097831e-01f,-4.052413106e-01f,9.129621983e-01f,-4.080441594e-01f,9.117060304e-01f,-4.108431637e-01f,9.104412794e-01f,-4.136383235e-01f,9.091680050e-01f,-4.164295495e-01f,9.078860879e-01f,-4.192169011e-01f,9.065957069e-01f,-4.220002592e-01f,9.052967429e-01f,-4.247796834e-01f,
9.039893150e-01f,-4.275550842e-01f,9.026733041e-01f,-4.303264916e-01f,9.013488293e-01f,-4.330938160e-01f,9.000158906e-01f,-4.358570874e-01f,8.986744881e-01f,-4.386162460e-01f,8.973245621e-01f,-4.413712621e-01f,8.959662318e-01f,-4.441221356e-01f,8.945994973e-01f,-4.468688369e-01f,
8.932242990e-01f,-4.496113360e-01f,8.918406963e-01f,-4.523495734e-01f,8.904487491e-01f,-4.550835788e-01f,8.890483379e-01f,-4.578132927e-01f,8.876396418e-01f,-4.605387151e-01f,8.862225413e-01f,-4.632597864e-01f,8.847970963e-01f,-4.659765065e-01f,8.833633661e-01f,-4.686888158e-01f,
8.819212914e-01f,-4.713967443e-01f,8.804708719e-01f,-4.741002023e-01f,8.790122271e-01f,-4.767992198e-01f,8.775452971e-01f,-4.794937670e-01f,8.760700822e-01f,-4.821837842e-01f,8.745866418e-01f,-4.848692417e-01f,8.730949759e-01f,-4.875501692e-01f,8.715950847e-01f,-4.902264774e-01f,
8.700869679e-01f,-4.928981960e-01f,8.685706854e-01f,-4.955652654e-01f,8.670462370e-01f,-4.982276559e-01f,8.655136228e-01f,-5.008853674e-01f,8.639728427e-01f,-5.035383701e-01f,8.624239564e-01f,-5.061866641e-01f,8.608669639e-01f,-5.088301301e-01f,8.593018055e-01f,-5.114688277e-01f,
8.577286005e-01f,-5.141027570e-01f,8.561473489e-01f,-5.167317986e-01f,8.545579910e-01f,-5.193560123e-01f,8.529605865e-01f,-5.219752789e-01f,8.513551950e-01f,-5.245896578e-01f,8.497417569e-01f,-5.271991491e-01f,8.481203318e-01f,-5.298036337e-01f,8.464909196e-01f,-5.324031115e-01f,
8.448535800e-01f,-5.349976420e-01f,8.432082534e-01f,-5.375870466e-01f,8.415549994e-01f,-5.401714444e-01f,8.398938179e-01f,-5.427507758e-01f,8.382247090e-01f,-5.453249812e-01f,8.365477324e-01f,-5.478940606e-01f,8.348628879e-01f,-5.504579544e-01f,8.331701756e-01f,-5.530167222e-01f,
8.314695954e-01f,-5.555702448e-01f,8.297612071e-01f,-5.581185222e-01f,8.280450702e-01f,-5.606615543e-01f,8.263210654e-01f,-5.631993413e-01f,8.245893121e-01f,-5.657318234e-01f,8.228498101e-01f,-5.682589412e-01f,8.211025000e-01f,-5.707807541e-01f,8.193475008e-01f,-5.732971430e-01f,
8.175848126e-01f,-5.758081675e-01f,8.158144355e-01f,-5.783137679e-01f,8.140363097e-01f,-5.808139443e-01f,8.122506142e-01f,-5.833086371e-01f,8.104571700e-01f,-5.857978463e-01f,8.086561561e-01f,-5.882815719e-01f,8.068475723e-01f,-5.907596946e-01f,8.050313592e-01f,-5.932322741e-01f,
8.032075167e-01f,-5.956993103e-01f,8.013761640e-01f,-5.981606841e-01f,7.995372415e-01f,-6.006164551e-01f,7.976908684e-01f,-6.030666232e-01f,7.958369255e-01f,-6.055110693e-01f,7.939754725e-01f,-6.079497933e-01f,7.921065688e-01f,-6.103827953e-01f,7.902302146e-01f,-6.128100753e-01f,
7.883464098e-01f,-6.152315736e-01f,7.864552140e-01f,-6.176472902e-01f,7.845565677e-01f,-6.200572252e-01f,7.826505899e-01f,-6.224612594e-01f,7.807372212e-01f,-6.248595119e-01f,7.788165212e-01f,-6.272518039e-01f,7.768884897e-01f,-6.296382546e-01f,7.749531269e-01f,-6.320187449e-01f,
7.730104327e-01f,-6.343932748e-01f,7.710605264e-01f,-6.367618442e-01f,7.691033483e-01f,-6.391244531e-01f,7.671388984e-01f,-6.414810419e-01f,7.651672363e-01f,-6.438315511e-01f,7.631884217e-01f,-6.461760402e-01f,7.612023950e-01f,-6.485143900e-01f,7.592092156e-01f,-6.508466601e-01f,
7.572088242e-01f,-6.531728506e-01f,7.552013993e-01f,-6.554928422e-01f,7.531868219e-01f,-6.578066945e-01f,7.511651516e-01f,-6.601143479e-01f,7.491363883e-01f,-6.624158025e-01f,7.471005917e-01f,-6.647109985e-01f,7.450577617e-01f,-6.669999361e-01f,7.430079579e-01f,-6.692826152e-01f,
7.409511209e-01f,-6.715589762e-01f,7.388873100e-01f,-6.738290191e-01f,7.368165851e-01f,-6.760926843e-01f,7.347388864e-01f,-6.783500314e-01f,7.326542735e-01f,-6.806010008e-01f,7.305627465e-01f,-6.828455329e-01f,7.284643650e-01f,-6.850836873e-01f,7.263591290e-01f,-6.873153448e-01f,
7.242470980e-01f,-6.895405650e-01f,7.221282125e-01f,-6.917592287e-01f,7.200025320e-01f,-6.939714551e-01f,7.178700566e-01f,-6.961771250e-01f,7.157308459e-01f,-6.983762383e-01f,7.135848403e-01f,-7.005687952e-01f,7.114322186e-01f,-7.027547359e-01f,7.092728019e-01f,-7.049340606e-01f,
7.071067691e-01f,-7.071067691e-01f,7.049340606e-01f,-7.092728019e-01f,7.027547359e-01f,-7.114322186e-01f,7.005687952e-01f,-7.135848403e-01f,6.983762383e-01f,-7.157308459e-01f,6.961771250e-01f,-7.178700566e-01f,6.939714551e-01f,-7.200025320e-01f,6.917592287e-01f,-7.221282125e-01f,
6.895405650e-01f,-7.242470980e-01f,6.873153448e-01f,-7.263591290e-01f,6.850836873e-01f,-7.284643650e-01f,6.828455329e-01f,-7.305627465e-01f,6.806010008e-01f,-7.326542735e-01f,6.783500314e-01f,-7.347388864e-01f,6.760926843e-01f,-7.368165851e-01f,6.738290191e-01f,-7.388873100e-01f,
6.715589762e-01f,-7.409511209e-01f,6.692826152e-01f,-7.430079579e-01f,6.669999361e-01f,-7.450577617e-01f,6.647109985e-01f,-7.471005917e-01f,6.624158025e-01f,-7.491363883e-01f,6.601143479e-01f,-7.511651516e-01f,6.578066945e-01f,-7.531868219e-01f,6.554928422e-01f,-7.552013993e-01f,
6.531728506e-01f,-7.572088242e-01f,6.508466601e-01f,-7.592092156e-01f,6.485143900e-01f,-7.612023950e-01f,6.461760402e-01f,-7.631884217e-01f,6.438315511e-01f,-7.651672363e-01f,6.414810419e-01f,-7.671388984e-01f,6.391244531e-01f,-7.691033483e-01f,6.367618442e-01f,-7.710605264e-01f,
6.343932748e-01f,-7.730104327e-01f,6.320187449e-01f,-7.749531269e-01f,6.296382546e-01f,-7.768884897e-01f,6.272518039e-01f,-7.788165212e-01f,6.248595119e-01f,-7.807372212e-01f,6.224612594e-01f,-7.826505899e-01f,6.200572252e-01f,-7.845565677e-01f,6.176472902e-01f,-7.864552140e-01f,
6.152315736e-01f,-7.883464098e-01f,6.128100753e-01f,-7.902302146e-01f,6.103827953e-01f,-7.921065688e-01f,6.079497933e-01f,-7.939754725e-01f,6.055110693e-01f,-7.958369255e-01f,6.030666232e-01f,-7.976908684e-01f,6.006164551e-01f,-7.995372415e-01f,5.981606841e-01f,-8.013761640e-01f,
5.956993103e-01f,-8.032075167e-01f,5.932322741e-01f,-8.050313592e-01f,5.907596946e-01f,-8.068475723e-01f,5.882815719e-01f,-8.086561561e-01f,5.857978463e-01f,-8.104571700e-01f,5.833086371e-01f,-8.122506142e-01f,5.808139443e-01f,-8.140363097e-01f,5.783137679e-01f,-8.158144355e-01f,
5.758081675e-01f,-8.175848126e-01f,5.732971430e-01f,-8.193475008e-01f,5.707807541e-01f,-8.211025000e-01f,5.682589412e-01f,-8.228498101e-01f,5.657318234e-01f,-8.245893121e-01f,5.631993413e-01f,-8.263210654e-01f,5.606615543e-01f,-8.280450702e-01f,5.581185222e-01f,-8.297612071e-01f,
5.555702448e-01f,-8.314695954e-01f,5.530167222e-01f,-8.331701756e-01f,5.504579544e-01f,-8.348628879e-01f,5.478940606e-01f,-8.365477324e-01f,5.453249812e-01f,-8.382247090e-01f,5.427507758e-01f,-8.398938179e-01f,5.401714444e-01f,-8.415549994e-01f,5.375870466e-01f,-8.432082534e-01f,
5.349976420e-01f,-8.448535800e-01f,5.324031115e-01f,-8.464909196e-01f,5.298036337e-01f,-8.481203318e-01f,5.271991491e-01f,-8.497417569e-01f,5.245896578e-01f,-8.513551950e-01f,5.219752789e-01f,-8.529605865e-01f,5.193560123e-01f,-8.545579910e-01f,5.167317986e-01f,-8.561473489e-01f,
5.141027570e-01f,-8.577286005e-01f,5.114688277e-01f,-8.593018055e-01f,5.088301301e-01f,-8.608669639e-01f,5.061866641e-01f,-8.624239564e-01f,5.035383701e-01f,-8.639728427e-01f,5.008853674e-01f,-8.655136228e-01f,4.982276559e-01f,-8.670462370e-01f,4.955652654e-01f,-8.685706854e-01f,
4.928981960e-01f,-8.700869679e-01f,4.902264774e-01f,-8.715950847e-01f,4.875501692e-01f,-8.730949759e-01f,4.848692417e-01f,-8.745866418e-01f,4.821837842e-01f,-8.760700822e-01f,4.794937670e-01f,-8.775452971e-01f,4.767992198e-01f,-8.790122271e-01f,4.741002023e-01f,-8.804708719e-01f,
4.713967443e-01f,-8.819212914e-01f,4.686888158e-01f,-8.833633661e-01f,4.659765065e-01f,-8.847970963e-01f,4.632597864e-01f,-8.862225413e-01f,4.605387151e-01f,-8.876396418e-01f,4.578132927e-01f,-8.890483379e-01f,4.550835788e-01f,-8.904487491e-01f,4.523495734e-01f,-8.918406963e-01f,
4.496113360e-01f,-8.932242990e-01f,4.468688369e-01f,-8.945994973e-01f,4.441221356e-01f,-8.959662318e-01f,4.413712621e-01f,-8.973245621e-01f,4.386162460e-01f,-8.986744881e-01f,4.358570874e-01f,-9.000158906e-01f,4.330938160e-01f,-9.013488293e-01f,4.303264916e-01f,-9.026733041e-01f,
4.275550842e-01f,-9.039893150e-01f,4.247796834e-01f,-9.052967429e-01f,4.220002592e-01f,-9.065957069e-01f,4.192169011e-01f,-9.078860879e-01f,4.164295495e-01f,-9.091680050e-01f,4.136383235e-01f,-9.104412794e-01f,4.108431637e-01f,-9.117060304e-01f,4.080441594e-01f,-9.129621983e-01f,
4.052413106e-01f,-9.142097831e-01f,4.024346471e-01f,-9.154487252e-01f,3.996241987e-01f,-9.166790843e-01f,3.968099952e-01f,-9.179008007e-01f,3.939920366e-01f,-9.191138744e-01f,3.911703825e-01f,-9.203183055e-01f,3.883450329e-01f,-9.215140343e-01f,3.855160475e-01f,-9.227011204e-01f,
3.826834261e-01f,-9.238795042e-01f,3.798471987e-01f,-9.250492454e-01f,3.770074248e-01f,-9.262102246e-01f,3.741640747e-01f,-9.273625016e-01f,3.713172078e-01f,-9.285060763e-01f,3.684668243e-01f,-9.296408892e-01f,3.656129837e-01f,-9.307669401e-01f,3.627557158e-01f,-9.318842888e-01f,
3.598950505e-01f,-9.329928160e-01f,3.570309579e-01f,-9.340925217e-01f,3.541635275e-01f,-9.351835251e-01f,3.512927592e-01f,-9.362656474e-01f,3.484186828e-01f,-9.373390079e-01f,3.455413282e-01f,-9.384035468e-01f,3.426607251e-01f,-9.394592047e-01f,3.397768736e-01f,-9.405060410e-01f,
3.368898630e-01f,-9.415440559e-01f,3.339996636e-01f,-9.425731897e-01f,3.311063051e-01f,-9.435934424e-01f,3.282098472e-01f,-9.446048141e-01f,3.253102899e-01f,-9.456073046e-01f,3.224076927e-01f,-9.466009140e-01f,3.195020258e-01f,-9.475855827e-01f,3.165933788e-01f,-9.485613704e-01f,
3.136817515e-01f,-9.495281577e-01f,3.107671440e-01f,-9.504860640e-01f,3.078496456e-01f,-9.514350295e-01f,3.049292266e-01f,-9.523749948e-01f,3.020059466e-01f,-9.533060193e-01f,2.990798354e-01f,-9.542281032e-01f,2.961508930e-01f,-9.551411867e-01f,2.932191491e-01f,-9.560452700e-01f,
2.902846634e-01f,-9.569403529e-01f,2.873474658e-01f,-9.578264356e-01f,2.844075263e-01f,-9.587034583e-01f,2.814649343e-01f,-9.595715404e-01f,2.785196900e-01f,-9.604305029e-01f,2.755718231e-01f,-9.612804651e-01f,2.726213634e-01f,-9.621214271e-01f,2.696683109e-01f,-9.629532695e-01f,
2.667127550e-01f,-9.637760520e-01f,2.637546659e-01f,-9.645897746e-01f,2.607941031e-01f,-9.653944373e-01f,2.578310966e-01f,-9.661899805e-01f,2.548656464e-01f,-9.669764638e-01f,2.518978119e-01f,-9.677538276e-01f,2.489276081e-01f,-9.685220718e-01f,2.459550500e-01f,-9.692812562e-01f,
2.429801822e-01f,-9.700312614e-01f,2.400030196e-01f,-9.707721472e-01f,2.370236069e-01f,-9.715039134e-01f,2.340419590e-01f,-9.722265005e-01f,2.310581058e-01f,-9.729399681e-01f,2.280720770e-01f,-9.736442566e-01f,2.250839174e-01f,-9.743393660e-01f,2.220936269e-01f,-9.750253558e-01f,
2.191012353e-01f,-9.757021070e-01f,2.161068022e-01f,-9.763697386e-01f,2.131103128e-01f,-9.770281315e-01f,2.101118416e-01f,-9.776773453e-01f,2.071113735e-01f,-9.783173800e-01f,2.041089684e-01f,-9.789481759e-01f,2.011046410e-01f,-9.795697927e-01f,1.980984062e-01f,-9.801821113e-01f,
1.950903237e-01f,-9.807852507e-01f,1.920803934e-01f,-9.813792109e-01f,1.890686601e-01f,-9.819638729e-01f,1.860551536e-01f,-9.825392962e-01f,1.830398887e-01f,-9.831054807e-01f,1.800228953e-01f,-9.836624265e-01f,1.770042181e-01f,-9.842100739e-01f,1.739838719e-01f,-9.847484827e-01f,
1.709618866e-01f,-9.852776527e-01f,1.679382920e-01f,-9.857975245e-01f,1.649131179e-01f,-9.863080978e-01f,1.618863940e-01f,-9.868093729e-01f,1.588581502e-01f,-9.873014092e-01f,1.558284014e-01f,-9.877841473e-01f,1.527971923e-01f,-9.882575870e-01f,1.497645378e-01f,-9.887216687e-01f,
1.467304677e-01f,-9.891765118e-01f,1.436950266e-01f,-9.896219969e-01f,1.406582445e-01f,-9.900581837e-01f,1.376201212e-01f,-9.904850721e-01f,1.345807016e-01f,-9.909026623e-01f,1.315400302e-01f,-9.913108349e-01f,1.284981072e-01f,-9.917097688e-01f,1.254549772e-01f,-9.920992851e-01f,
1.224106774e-01f,-9.924795628e-01f,1.193652153e-01f,-9.928504229e-01f,1.163186282e-01f,-9.932119250e-01f,1.132709533e-01f,-9.935641289e-01f,1.102222055e-01f,-9.939069748e-01f,1.071724221e-01f,-9.942404628e-01f,1.041216329e-01f,-9.945645928e-01f,1.010698602e-01f,-9.948793054e-01f,
9.801714122e-02f,-9.951847196e-01f,9.496349841e-02f,-9.954807758e-01f,9.190895408e-02f,-9.957674146e-01f,8.885355294e-02f,-9.960446954e-01f,8.579730988e-02f,-9.963126183e-01f,8.274026215e-02f,-9.965711236e-01f,7.968243957e-02f,-9.968202710e-01f,7.662386447e-02f,-9.970600605e-01f,
7.356456667e-02f,-9.972904325e-01f,7.050457597e-02f,-9.975114465e-01f,6.744392216e-02f,-9.977230430e-01f,6.438262761e-02f,-9.979252815e-01f,6.132073700e-02f,-9.981181026e-01f,5.825826526e-02f,-9.983015656e-01f,5.519524589e-02f,-9.984755516e-01f,5.213170499e-02f,-9.986402392e-01f,
4.906767607e-02f,-9.987954497e-01f,4.600318149e-02f,-9.989413023e-01f,4.293825850e-02f,-9.990777373e-01f,3.987292573e-02f,-9.992047548e-01f,3.680722415e-02f,-9.993223548e-01f,3.374117240e-02f,-9.994305968e-01f,3.067480400e-02f,-9.995294213e-01f,2.760814503e-02f,-9.996188283e-01f,
2.454122901e-02f,-9.996988177e-01f,2.147408016e-02f,-9.997693896e-01f,1.840673015e-02f,-9.998306036e-01f,1.533920597e-02f,-9.998823404e-01f,1.227153838e-02f,-9.999247193e-01f,9.203754365e-03f,-9.999576211e-01f,6.135884672e-03f,-9.999811649e-01f,3.067956772e-03f,-9.999952912e-01f,
6.123234263e-17f,-1.000000000e+00f,-3.067956772e-03f,-9.999952912e-01f,-6.135884672e-03f,-9.999811649e-01f,-9.203754365e-03f,-9.999576211e-01f,-1.227153838e-02f,-9.999247193e-01f,-1.533920597e-02f,-9.998823404e-01f,-1.840673015e-02f,-9.998306036e-01f,-2.147408016e-02f,-9.997693896e-01f,
-2.454122901e-02f,-9.996988177e-01f,-2.760814503e-02f,-9.996188283e-01f,-3.067480400e-02f,-9.995294213e-01f,-3.374117240e-02f,-9.994305968e-01f,-3.680722415e-02f,-9.993223548e-01f,-3.987292573e-02f,-9.992047548e-01f,-4.293825850e-02f,-9.990777373e-01f,-4.600318149e-02f,-9.989413023e-01f,
-4.906767607e-02f,-9.987954497e-01f,-5.213170499e-02f,-9.986402392e-01f,-5.519524589e-02f,-9.984755516e-01f,-5.825826526e-02f,-9.983015656e-01f,-6.132073700e-02f,-9.981181026e-01f,-6.438262761e-02f,-9.979252815e-01f,-6.744392216e-02f,-9.977230430e-01f,-7.050457597e-02f,-9.975114465e-01f,
-7.356456667e-02f,-9.972904325e-01f,-7.662386447e-02f,-9.970600605e-01f,-7.968243957e-02f,-9.968202710e-01f,-8.274026215e-02f,-9.965711236e-01f,-8.579730988e-02f,-9.963126183e-01f,-8.885355294e-02f,-9.960446954e-01f,-9.190895408e-02f,-9.957674146e-01f,-9.496349841e-02f,-9.954807758e-01f,
-9.801714122e-02f,-9.951847196e-01f,-1.010698602e-01f,-9.948793054e-01f,-1.041216329e-01f,-9.945645928e-01f,-1.071724221e-01f,-9.942404628e-01f,-1.102222055e-01f,-9.939069748e-01f,-1.132709533e-01f,-9.935641289e-01f,-1.163186282e-01f,-9.932119250e-01f,-1.193652153e-01f,-9.928504229e-01f,
-1.224106774e-01f,-9.924795628e-01f,-1.254549772e-01f,-9.920992851e-01f,-1.284981072e-01f,-9.917097688e-01f,-1.315400302e-01f,-9.913108349e-01f,-1.345807016e-01f,-9.909026623e-01f,-1.376201212e-01f,-9.904850721e-01f,-1.406582445e-01f,-9.900581837e-01f,-1.436950266e-01f,-9.896219969e-01f,
-1.467304677e-01f,-9.891765118e-01f,-1.497645378e-01f,-9.887216687e-01f,-1.527971923e-01f,-9.882575870e-01f,-1.558284014e-01f,-9.877841473e-01f,-1.588581502e-01f,-9.873014092e-01f,-1.618863940e-01f,-9.868093729e-01f,-1.649131179e-01f,-9.863080978e-01f,-1.679382920e-01f,-9.857975245e-01f,
-1.709618866e-01f,-9.852776527e-01f,-1.739838719e-01f,-9.847484827e-01f,-1.770042181e-01f,-9.842100739e-01f,-1.800228953e-01f,-9.836624265e-01f,-1.830398887e-01f,-9.831054807e-01f,-1.860551536e-01f,-9.825392962e-01f,-1.890686601e-01f,-9.819638729e-01f,-1.920803934e-01f,-9.813792109e-01f,
-1.950903237e-01f,-9.807852507e-01f,-1.980984062e-01f,-9.801821113e-01f,-2.011046410e-01f,-9.795697927e-01f,-2.041089684e-01f,-9.789481759e-01f,-2.071113735e-01f,-9.783173800e-01f,-2.101118416e-01f,-9.776773453e-01f,-2.131103128e-01f,-9.770281315e-01f,-2.161068022e-01f,-9.763697386e-01f,
-2.191012353e-01f,-9.757021070e-01f,-2.220936269e-01f,-9.750253558e-01f,-2.250839174e-01f,-9.743393660e-01f,-2.280720770e-01f,-9.736442566e-01f,-2.310581058e-01f,-9.729399681e-01f,-2.340419590e-01f,-9.722265005e-01f,-2.370236069e-01f,-9.715039134e-01f,-2.400030196e-01f,-9.707721472e-01f,
-2.429801822e-01f,-9.700312614e-01f,-2.459550500e-01f,-9.692812562e-01f,-2.489276081e-01f,-9.685220718e-01f,-2.518978119e-01f,-9.677538276e-01f,-2.548656464e-01f,-9.669764638e-01f,-2.578310966e-01f,-9.661899805e-01f,-2.607941031e-01f,-9.653944373e-01f,-2.637546659e-01f,-9.645897746e-01f,
-2.667127550e-01f,-9.637760520e-01f,-2.696683109e-01f,-9.629532695e-01f,-2.726213634e-01f,-9.621214271e-01f,-2.755718231e-01f,-9.612804651e-01f,-2.785196900e-01f,-9.604305029e-01f,-2.814649343e-01f,-9.595715404e-01f,-2.844075263e-01f,-9.587034583e-01f,-2.873474658e-01f,-9.578264356e-01f,
-2.902846634e-01f,-9.569403529e-01f,-2.932191491e-01f,-9.560452700e-01f,-2.961508930e-01f,-9.551411867e-01f,-2.990798354e-01f,-9.542281032e-01f,-3.020059466e-01f,-9.533060193e-01f,-3.049292266e-01f,-9.523749948e-01f,-3.078496456e-01f,-9.514350295e-01f,-3.107671440e-01f,-9.504860640e-01f,
-3.136817515e-01f,-9.495281577e-01f,-3.165933788e-01f,-9.485613704e-01f,-3.195020258e-01f,-9.475855827e-01f,-3.224076927e-01f,-9.466009140e-01f,-3.253102899e-01f,-9.456073046e-01f,-3.282098472e-01f,-9.446048141e-01f,-3.311063051e-01f,-9.435934424e-01f,-3.339996636e-01f,-9.425731897e-01f,
-3.368898630e-01f,-9.415440559e-01f,-3.397768736e-01f,-9.405060410e-01f,-3.426607251e-01f,-9.394592047e-01f,-3.455413282e-01f,-9.384035468e-01f,-3.484186828e-01f,-9.373390079e-01f,-3.512927592e-01f,-9.362656474e-01f,-3.541635275e-01f,-9.351835251e-01f,-3.570309579e-01f,-9.340925217e-01f,
-3.598950505e-01f,-9.329928160e-01f,-3.627557158e-01f,-9.318842888e-01f,-3.656129837e-01f,-9.307669401e-01f,-3.684668243e-01f,-9.296408892e-01f,-3.713172078e-01f,-9.285060763e-01f,-3.741640747e-01f,-9.273625016e-01f,-3.770074248e-01f,-9.262102246e-01f,-3.798471987e-01f,-9.250492454e-01f,
-3.826834261e-01f,-9.238795042e-01f,-3.855160475e-01f,-9.227011204e-01f,-3.883450329e-01f,-9.215140343e-01f,-3.911703825e-01f,-9.203183055e-01f,-3.939920366e-01f,-9.191138744e-01f,-3.968099952e-01f,-9.179008007e-01f,-3.996241987e-01f,-9.166790843e-01f,-4.024346471e-01f,-9.154487252e-01f,
-4.052413106e-01f,-9.142097831e-01f,-4.080441594e-01f,-9.129621983e-01f,-4.108431637e-01f,-9.117060304e-01f,-4.136383235e-01f,-9.104412794e-01f,-4.164295495e-01f,-9.091680050e-01f,-4.192169011e-01f,-9.078860879e-01f,-4.220002592e-01f,-9.065957069e-01f,-4.247796834e-01f,-9.052967429e-01f,
-4.275550842e-01f,-9.039893150e-01f,-4.303264916e-01f,-9.026733041e-01f,-4.330938160e-01f,-9.013488293e-01f,-4.358570874e-01f,-9.000158906e-01f,-4.386162460e-01f,-8.986744881e-01f,-4.413712621e-01f,-8.973245621e-01f,-4.441221356e-01f,-8.959662318e-01f,-4.468688369e-01f,-8.945994973e-01f,
-4.496113360e-01f,-8.932242990e-01f,-4.523495734e-01f,-8.918406963e-01f,-4.550835788e-01f,-8.904487491e-01f,-4.578132927e-01f,-8.890483379e-01f,-4.605387151e-01f,-8.876396418e-01f,-4.632597864e-01f,-8.862225413e-01f,-4.659765065e-01f,-8.847970963e-01f,-4.686888158e-01f,-8.833633661e-01f,
-4.713967443e-01f,-8.819212914e-01f,-4.741002023e-01f,-8.804708719e-01f,-4.767992198e-01f,-8.790122271e-01f,-4.794937670e-01f,-8.775452971e-01f,-4.821837842e-01f,-8.760700822e-01f,-4.848692417e-01f,-8.745866418e-01f,-4.875501692e-01f,-8.730949759e-01f,-4.902264774e-01f,-8.715950847e-01f,
-4.928981960e-01f,-8.700869679e-01f,-4.955652654e-01f,-8.685706854e-01f,-4.982276559e-01f,-8.670462370e-01f,-5.008853674e-01f,-8.655136228e-01f,-5.035383701e-01f,-8.639728427e-01f,-5.061866641e-01f,-8.624239564e-01f,-5.088301301e-01f,-8.608669639e-01f,-5.114688277e-01f,-8.593018055e-01f,
-5.141027570e-01f,-8.577286005e-01f,-5.167317986e-01f,-8.561473489e-01f,-5.193560123e-01f,-8.545579910e-01f,-5.219752789e-01f,-8.529605865e-01f,-5.245896578e-01f,-8.513551950e-01f,-5.271991491e-01f,-8.497417569e-01f,-5.298036337e-01f,-8.481203318e-01f,-5.324031115e-01f,-8.464909196e-01f,
-5.349976420e-01f,-8.448535800e-01f,-5.375870466e-01f,-8.432082534e-01f,-5.401714444e-01f,-8.415549994e-01f,-5.427507758e-01f,-8.398938179e-01f,-5.453249812e-01f,-8.382247090e-01f,-5.478940606e-01f,-8.365477324e-01f,-5.504579544e-01f,-8.348628879e-01f,-5.530167222e-01f,-8.331701756e-01f,
-5.555702448e-01f,-8.314695954e-01f,-5.581185222e-01f,-8.297612071e-01f,-5.606615543e-01f,-8.280450702e-01f,-5.631993413e-01f,-8.263210654e-01f,-5.657318234e-01f,-8.245893121e-01f,-5.682589412e-01f,-8.228498101e-01f,-5.707807541e-01f,-8.211025000e-01f,-5.732971430e-01f,-8.193475008e-01f,
-5.758081675e-01f,-8.175848126e-01f,-5.783137679e-01f,-8.158144355e-01f,-5.808139443e-01f,-8.140363097e-01f,-5.833086371e-01f,-8.122506142e-01f,-5.857978463e-01f,-8.104571700e-01f,-5.882815719e-01f,-8.086561561e-01f,-5.907596946e-01f,-8.068475723e-01f,-5.932322741e-01f,-8.050313592e-01f,
-5.956993103e-01f,-8.032075167e-01f,-5.981606841e-01f,-8.013761640e-01f,-6.006164551e-01f,-7.995372415e-01f,-6.030666232e-01f,-7.976908684e-01f,-6.055110693e-01f,-7.958369255e-01f,-6.079497933e-01f,-7.939754725e-01f,-6.103827953e-01f,-7.921065688e-01f,-6.128100753e-01f,-7.902302146e-01f,
-6.152315736e-01f,-7.883464098e-01f,-6.176472902e-01f,-7.864552140e-01f,-6.200572252e-01f,-7.845565677e-01f,-6.224612594e-01f,-7.826505899e-01f,-6.248595119e-01f,-7.807372212e-01f,-6.272518039e-01f,-7.788165212e-01f,-6.296382546e-01f,-7.768884897e-01f,-6.320187449e-01f,-7.749531269e-01f,
-6.343932748e-01f,-7.730104327e-01f,-6.367618442e-01f,-7.710605264e-01f,-6.391244531e-01f,-7.691033483e-01f,-6.414810419e-01f,-7.671388984e-01f,-6.438315511e-01f,-7.651672363e-01f,-6.461760402e-01f,-7.631884217e-01f,-6.485143900e-01f,-7.612023950e-01f,-6.508466601e-01f,-7.592092156e-01f,
-6.531728506e-01f,-7.572088242e-01f,-6.554928422e-01f,-7.552013993e-01f,-6.578066945e-01f,-7.531868219e-01f,-6.601143479e-01f,-7.511651516e-01f,-6.624158025e-01f,-7.491363883e-01f,-6.647109985e-01f,-7.471005917e-01f,-6.669999361e-01f,-7.450577617e-01f,-6.692826152e-01f,-7.430079579e-01f,
-6.715589762e-01f,-7.409511209e-01f,-6.738290191e-01f,-7.388873100e-01f,-6.760926843e-01f,-7.368165851e-01f,-6.783500314e-01f,-7.347388864e-01f,-6.806010008e-01f,-7.326542735e-01f,-6.828455329e-01f,-7.305627465e-01f,-6.850836873e-01f,-7.284643650e-01f,-6.873153448e-01f,-7.263591290e-01f,
-6.895405650e-01f,-7.242470980e-01f,-6.917592287e-01f,-7.221282125e-01f,-6.939714551e-01f,-7.200025320e-01f,-6.961771250e-01f,-7.178700566e-01f,-6.983762383e-01f,-7.157308459e-01f,-7.005687952e-01f,-7.135848403e-01f,-7.027547359e-01f,-7.114322186e-01f,-7.049340606e-01f,-7.092728019e-01f,
-7.071067691e-01f,-7.071067691e-01f,-7.092728019e-01f,-7.049340606e-01f,-7.114322186e-01f,-7.027547359e-01f,-7.135848403e-01f,-7.005687952e-01f,-7.157308459e-01f,-6.983762383e-01f,-7.178700566e-01f,-6.961771250e-01f,-7.200025320e-01f,-6.939714551e-01f,-7.221282125e-01f,-6.917592287e-01f,
-7.242470980e-01f,-6.895405650e-01f,-7.263591290e-01f,-6.873153448e-01f,-7.284643650e-01f,-6.850836873e-01f,-7.305627465e-01f,-6.828455329e-01f,-7.326542735e-01f,-6.806010008e-01f,-7.347388864e-01f,-6.783500314e-01f,-7.368165851e-01f,-6.760926843e-01f,-7.388873100e-01f,-6.738290191e-01f,
-7.409511209e-01f,-6.715589762e-01f,-7.430079579e-01f,-6.692826152e-01f,-7.450577617e-01f,-6.669999361e-01f,-7.471005917e-01f,-6.647109985e-01f,-7.491363883e-01f,-6.624158025e-01f,-7.511651516e-01f,-6.601143479e-01f,-7.531868219e-01f,-6.578066945e-01f,-7.552013993e-01f,-6.554928422e-01f,
-7.572088242e-01f,-6.531728506e-01f,-7.592092156e-01f,-6.508466601e-01f,-7.612023950e-01f,-6.485143900e-01f,-7.631884217e-01f,-6.461760402e-01f,-7.651672363e-01f,-6.438315511e-01f,-7.671388984e-01f,-6.414810419e-01f,-7.691033483e-01f,-6.391244531e-01f,-7.710605264e-01f,-6.367618442e-01f,
-7.730104327e-01f,-6.343932748e-01f,-7.749531269e-01f,-6.320187449e-01f,-7.768884897e-01f,-6.296382546e-01f,-7.788165212e-01f,-6.272518039e-01f,-7.807372212e-01f,-6.248595119e-01f,-7.826505899e-01f,-6.224612594e-01f,-7.845565677e-01f,-6.200572252e-01f,-7.864552140e-01f,-6.176472902e-01f,
-7.883464098e-01f,-6.152315736e-01f,-7.902302146e-01f,-6.128100753e-01f,-7.921065688e-01f,-6.103827953e-01f,-7.939754725e-01f,-6.079497933e-01f,-7.958369255e-01f,-6.055110693e-01f,-7.976908684e-01f,-6.030666232e-01f,-7.995372415e-01f,-6.006164551e-01f,-8.013761640e-01f,-5.981606841e-01f,
-8.032075167e-01f,-5.956993103e-01f,-8.050313592e-01f,-5.932322741e-01f,-8.068475723e-01f,-5.907596946e-01f,-8.086561561e-01f,-5.882815719e-01f,-8.104571700e-01f,-5.857978463e-01f,-8.122506142e-01f,-5.833086371e-01f,-8.140363097e-01f,-5.808139443e-01f,-8.158144355e-01f,-5.783137679e-01f,
-8.175848126e-01f,-5.758081675e-01f,-8.193475008e-01f,-5.732971430e-01f,-8.211025000e-01f,-5.707807541e-01f,-8.228498101e-01f,-5.682589412e-01f,-8.245893121e-01f,-5.657318234e-01f,-8.263210654e-01f,-5.631993413e-01f,-8.280450702e-01f,-5.606615543e-01f,-8.297612071e-01f,-5.581185222e-01f,
-8.314695954e-01f,-5.555702448e-01f,-8.331701756e-01f,-5.530167222e-01f,-8.348628879e-01f,-5.504579544e-01f,-8.365477324e-01f,-5.478940606e-01f,-8.382247090e-01f,-5.453249812e-01f,-8.398938179e-01f,-5.427507758e-01f,-8.415549994e-01f,-5.401714444e-01f,-8.432082534e-01f,-5.375870466e-01f,
-8.448535800e-01f,-5.349976420e-01f,-8.464909196e-01f,-5.324031115e-01f,-8.481203318e-01f,-5.298036337e-01f,-8.497417569e-01f,-5.271991491e-01f,-8.513551950e-01f,-5.245896578e-01f,-8.529605865e-01f,-5.219752789e-01f,-8.545579910e-01f,-5.193560123e-01f,-8.561473489e-01f,-5.167317986e-01f,
-8.577286005e-01f,-5.141027570e-01f,-8.593018055e-01f,-5.114688277e-01f,-8.608669639e-01f,-5.088301301e-01f,-8.624239564e-01f,-5.061866641e-01f,-8.639728427e-01f,-5.035383701e-01f,-8.655136228e-01f,-5.008853674e-01f,-8.670462370e-01f,-4.982276559e-01f,-8.685706854e-01f,-4.955652654e-01f,
-8.700869679e-01f,-4.928981960e-01f,-8.715950847e-01f,-4.902264774e-01f,-8.730949759e-01f,-4.875501692e-01f,-8.745866418e-01f,-4.848692417e-01f,-8.760700822e-01f,-4.821837842e-01f,-8.775452971e-01f,-4.794937670e-01f,-8.790122271e-01f,-4.767992198e-01f,-8.804708719e-01f,-4.741002023e-01f,
-8.819212914e-01f,-4.713967443e-01f,-8.833633661e-01f,-4.686888158e-01f,-8.847970963e-01f,-4.659765065e-01f,-8.862225413e-01f,-4.632597864e-01f,-8.876396418e-01f,-4.605387151e-01f,-8.890483379e-01f,-4.578132927e-01f,-8.904487491e-01f,-4.550835788e-01f,-8.918406963e-01f,-4.523495734e-01f,
-8.932242990e-01f,-4.496113360e-01f,-8.945994973e-01f,-4.468688369e-01f,-8.959662318e-01f,-4.441221356e-01f,-8.973245621e-01f,-4.413712621e-01f,-8.986744881e-01f,-4.386162460e-01f,-9.000158906e-01f,-4.358570874e-01f,-9.013488293e-01f,-4.330938160e-01f,-9.026733041e-01f,-4.303264916e-01f,
-9.039893150e-01f,-4.275550842e-01f,-9.052967429e-01f,-4.247796834e-01f,-9.065957069e-01f,-4.220002592e-01f,-9.078860879e-01f,-4.192169011e-01f,-9.091680050e-01f,-4.164295495e-01f,-9.104412794e-01f,-4.136383235e-01f,-9.117060304e-01f,-4.108431637e-01f,-9.129621983e-01f,-4.080441594e-01f,
-9.142097831e-01f,-4.052413106e-01f,-9.154487252e-01f,-4.024346471e-01f,-9.166790843e-01f,-3.996241987e-01f,-9.179008007e-01f,-3.968099952e-01f,-9.191138744e-01f,-3.939920366e-01f,-9.203183055e-01f,-3.911703825e-01f,-9.215140343e-01f,-3.883450329e-01f,-9.227011204e-01f,-3.855160475e-01f,
-9.238795042e-01f,-3.826834261e-01f,-9.250492454e-01f,-3.798471987e-01f,-9.262102246e-01f,-3.770074248e-01f,-9.273625016e-01f,-3.741640747e-01f,-9.285060763e-01f,-3.713172078e-01f,-9.296408892e-01f,-3.684668243e-01f,-9.307669401e-01f,-3.656129837e-01f,-9.318842888e-01f,-3.627557158e-01f,
-9.329928160e-01f,-3.598950505e-01f,-9.340925217e-01f,-3.570309579e-01f,-9.351835251e-01f,-3.541635275e-01f,-9.362656474e-01f,-3.512927592e-01f,-9.373390079e-01f,-3.484186828e-01f,-9.384035468e-01f,-3.455413282e-01f,-9.394592047e-01f,-3.426607251e-01f,-9.405060410e-01f,-3.397768736e-01f,
-9.415440559e-01f,-3.368898630e-01f,-9.425731897e-01f,-3.339996636e-01f,-9.435934424e-01f,-3.311063051e-01f,-9.446048141e-01f,-3.282098472e-01f,-9.456073046e-01f,-3.253102899e-01f,-9.466009140e-01f,-3.224076927e-01f,-9.475855827e-01f,-3.195020258e-01f,-9.485613704e-01f,-3.165933788e-01f,
-9.495281577e-01f,-3.136817515e-01f,-9.504860640e-01f,-3.107671440e-01f,-9.514350295e-01f,-3.078496456e-01f,-9.523749948e-01f,-3.049292266e-01f,-9.533060193e-01f,-3.020059466e-01f,-9.542281032e-01f,-2.990798354e-01f,-9.551411867e-01f,-2.961508930e-01f,-9.560452700e-01f,-2.932191491e-01f,
-9.569403529e-01f,-2.902846634e-01f,-9.578264356e-01f,-2.873474658e-01f,-9.587034583e-01f,-2.844075263e-01f,-9.595715404e-01f,-2.814649343e-01f,-9.604305029e-01f,-2.785196900e-01f,-9.612804651e-01f,-2.755718231e-01f,-9.621214271e-01f,-2.726213634e-01f,-9.629532695e-01f,-2.696683109e-01f,
-9.637760520e-01f,-2.667127550e-01f,-9.645897746e-01f,-2.637546659e-01f,-9.653944373e-01f,-2.607941031e-01f,-9.661899805e-01f,-2.578310966e-01f,-9.669764638e-01f,-2.548656464e-01f,-9.677538276e-01f,-2.518978119e-01f,-9.685220718e-01f,-2.489276081e-01f,-9.692812562e-01f,-2.459550500e-01f,
-9.700312614e-01f,-2.429801822e-01f,-9.707721472e-01f,-2.400030196e-01f,-9.715039134e-01f,-2.370236069e-01f,-9.722265005e-01f,-2.340419590e-01f,-9.729399681e-01f,-2.310581058e-01f,-9.736442566e-01f,-2.280720770e-01f,-9.743393660e-01f,-2.250839174e-01f,-9.750253558e-01f,-2.220936269e-01f,
-9.757021070e-01f,-2.191012353e-01f,-9.763697386e-01f,-2.161068022e-01f,-9.770281315e-01f,-2.131103128e-01f,-9.776773453e-01f,-2.101118416e-01f,-9.783173800e-01f,-2.071113735e-01f,-9.789481759e-01f,-2.041089684e-01f,-9.795697927e-01f,-2.011046410e-01f,-9.801821113e-01f,-1.980984062e-01f,
-9.807852507e-01f,-1.950903237e-01f,-9.813792109e-01f,-1.920803934e-01f,-9.819638729e-01f,-1.890686601e-01f,-9.825392962e-01f,-1.860551536e-01f,-9.831054807e-01f,-1.830398887e-01f,-9.836624265e-01f,-1.800228953e-01f,-9.842100739e-01f,-1.770042181e-01f,-9.847484827e-01f,-1.739838719e-01f,
-9.852776527e-01f,-1.709618866e-01f,-9.857975245e-01f,-1.679382920e-01f,-9.863080978e-01f,-1.649131179e-01f,-9.868093729e-01f,-1.618863940e-01f,-9.873014092e-01f,-1.588581502e-01f,-9.877841473e-01f,-1.558284014e-01f,-9.882575870e-01f,-1.527971923e-01f,-9.887216687e-01f,-1.497645378e-01f,
-9.891765118e-01f,-1.467304677e-01f,-9.896219969e-01f,-1.436950266e-01f,-9.900581837e-01f,-1.406582445e-01f,-9.904850721e-01f,-1.376201212e-01f,-9.909026623e-01f,-1.345807016e-01f,-9.913108349e-01f,-1.315400302e-01f,-9.917097688e-01f,-1.284981072e-01f,-9.920992851e-01f,-1.254549772e-01f,
-9.924795628e-01f,-1.224106774e-01f,-9.928504229e-01f,-1.193652153e-01f,-9.932119250e-01f,-1.163186282e-01f,-9.935641289e-01f,-1.132709533e-01f,-9.939069748e-01f,-1.102222055e-01f,-9.942404628e-01f,-1.071724221e-01f,-9.945645928e-01f,-1.041216329e-01f,-9.948793054e-01f,-1.010698602e-01f,
-9.951847196e-01f,-9.801714122e-02f,-9.954807758e-01f,-9.496349841e-02f,-9.957674146e-01f,-9.190895408e-02f,-9.960446954e-01f,-8.885355294e-02f,-9.963126183e-01f,-8.579730988e-02f,-9.965711236e-01f,-8.274026215e-02f,-9.968202710e-01f,-7.968243957e-02f,-9.970600605e-01f,-7.662386447e-02f,
-9.972904325e-01f,-7.356456667e-02f,-9.975114465e-01f,-7.050457597e-02f,-9.977230430e-01f,-6.744392216e-02f,-9.979252815e-01f,-6.438262761e-02f,-9.981181026e-01f,-6.132073700e-02f,-9.983015656e-01f,-5.825826526e-02f,-9.984755516e-01f,-5.519524589e-02f,-9.986402392e-01f,-5.213170499e-02f,
-9.987954497e-01f,-4.906767607e-02f,-9.989413023e-01f,-4.600318149e-02f,-9.990777373e-01f,-4.293825850e-02f,-9.992047548e-01f,-3.987292573e-02f,-9.993223548e-01f,-3.680722415e-02f,-9.994305968e-01f,-3.374117240e-02f,-9.995294213e-01f,-3.067480400e-02f,-9.996188283e-01f,-2.760814503e-02f,
-9.996988177e-01f,-2.454122901e-02f,-9.997693896e-01f,-2.147408016e-02f,-9.998306036e-01f,-1.840673015e-02f,-9.998823404e-01f,-1.533920597e-02f,-9.999247193e-01f,-1.227153838e-02f,-9.999576211e-01f,-9.203754365e-03f,-9.999811649e-01f,-6.135884672e-03f,-9.999952912e-01f,-3.067956772e-03f,
-1.000000000e+00f,-1.224646853e-16f,-9.999952912e-01f,3.067956772e-03f,-9.999811649e-01f,6.135884672e-03f,-9.999576211e-01f,9.203754365e-03f,-9.999247193e-01f,1.227153838e-02f,-9.998823404e-01f,1.533920597e-02f,-9.998306036e-01f,1.840673015e-02f,-9.997693896e-01f,2.147408016e-02f,
-9.996988177e-01f,2.454122901e-02f,-9.996188283e-01f,2.760814503e-02f,-9.995294213e-01f,3.067480400e-02f,-9.994305968e-01f,3.374117240e-02f,-9.993223548e-01f,3.680722415e-02f,-9.992047548e-01f,3.987292573e-02f,-9.990777373e-01f,4.293825850e-02f,-9.989413023e-01f,4.600318149e-02f,
-9.987954497e-01f,4.906767607e-02f,-9.986402392e-01f,5.213170499e-02f,-9.984755516e-01f,5.519524589e-02f,-9.983015656e-01f,5.825826526e-02f,-9.981181026e-01f,6.132073700e-02f,-9.979252815e-01f,6.438262761e-02f,-9.977230430e-01f,6.744392216e-02f,-9.975114465e-01f,7.050457597e-02f,
-9.972904325e-01f,7.356456667e-02f,-9.970600605e-01f,7.662386447e-02f,-9.968202710e-01f,7.968243957e-02f,-9.965711236e-01f,8.274026215e-02f,-9.963126183e-01f,8.579730988e-02f,-9.960446954e-01f,8.885355294e-02f,-9.957674146e-01f,9.190895408e-02f,-9.954807758e-01f,9.496349841e-02f,
-9.951847196e-01f,9.801714122e-02f,-9.948793054e-01f,1.010698602e-01f,-9.945645928e-01f,1.041216329e-01f,-9.942404628e-01f,1.071724221e-01f,-9.939069748e-01f,1.102222055e-01f,-9.935641289e-01f,1.132709533e-01f,-9.932119250e-01f,1.163186282e-01f,-9.928504229e-01f,1.193652153e-01f,
-9.924795628e-01f,1.224106774e-01f,-9.920992851e-01f,1.254549772e-01f,-9.917097688e-01f,1.284981072e-01f,-9.913108349e-01f,1.315400302e-01f,-9.909026623e-01f,1.345807016e-01f,-9.904850721e-01f,1.376201212e-01f,-9.900581837e-01f,1.406582445e-01f,-9.896219969e-01f,1.436950266e-01f,
-9.891765118e-01f,1.467304677e-01f,-9.887216687e-01f,1.497645378e-01f,-9.882575870e-01f,1.527971923e-01f,-9.877841473e-01f,1.558284014e-01f,-9.873014092e-01f,1.588581502e-01f,-9.868093729e-01f,1.618863940e-01f,-9.863080978e-01f,1.649131179e-01f,-9.857975245e-01f,1.679382920e-01f,
-9.852776527e-01f,1.709618866e-01f,-9.847484827e-01f,1.739838719e-01f,-9.842100739e-01f,1.770042181e-01f,-9.836624265e-01f,1.800228953e-01f,-9.831054807e-01f,1.830398887e-01f,-9.825392962e-01f,1.860551536e-01f,-9.819638729e-01f,1.890686601e-01f,-9.813792109e-01f,1.920803934e-01f,
-9.807852507e-01f,1.950903237e-01f,-9.801821113e-01f,1.980984062e-01f,-9.795697927e-01f,2.011046410e-01f,-9.789481759e-01f,2.041089684e-01f,-9.783173800e-01f,2.071113735e-01f,-9.776773453e-01f,2.101118416e-01f,-9.770281315e-01f,2.131103128e-01f,-9.763697386e-01f,2.161068022e-01f,
-9.757021070e-01f,2.191012353e-01f,-9.750253558e-01f,2.220936269e-01f,-9.743393660e-01f,2.250839174e-01f,-9.736442566e-01f,2.280720770e-01f,-9.729399681e-01f,2.310581058e-01f,-9.722265005e-01f,2.340419590e-01f,-9.715039134e-01f,2.370236069e-01f,-9.707721472e-01f,2.400030196e-01f,
-9.700312614e-01f,2.429801822e-01f,-9.692812562e-01f,2.459550500e-01f,-9.685220718e-01f,2.489276081e-01f,-9.677538276e-01f,2.518978119e-01f,-9.669764638e-01f,2.548656464e-01f,-9.661899805e-01f,2.578310966e-01f,-9.653944373e-01f,2.607941031e-01f,-9.645897746e-01f,2.637546659e-01f,
-9.637760520e-01f,2.667127550e-01f,-9.629532695e-01f,2.696683109e-01f,-9.621214271e-01f,2.726213634e-01f,-9.612804651e-01f,2.755718231e-01f,-9.604305029e-01f,2.785196900e-01f,-9.595715404e-01f,2.814649343e-01f,-9.587034583e-01f,2.844075263e-01f,-9.578264356e-01f,2.873474658e-01f,
-9.569403529e-01f,2.902846634e-01f,-9.560452700e-01f,2.932191491e-01f,-9.551411867e-01f,2.961508930e-01f,-9.542281032e-01f,2.990798354e-01f,-9.533060193e-01f,3.020059466e-01f,-9.523749948e-01f,3.049292266e-01f,-9.514350295e-01f,3.078496456e-01f,-9.504860640e-01f,3.107671440e-01f,
-9.495281577e-01f,3.136817515e-01f,-9.485613704e-01f,3.165933788e-01f,-9.475855827e-01f,3.195020258e-01f,-9.466009140e-01f,3.224076927e-01f,-9.456073046e-01f,3.253102899e-01f,-9.446048141e-01f,3.282098472e-01f,-9.435934424e-01f,3.311063051e-01f,-9.425731897e-01f,3.339996636e-01f,
-9.415440559e-01f,3.368898630e-01f,-9.405060410e-01f,3.397768736e-01f,-9.394592047e-01f,3.426607251e-01f,-9.384035468e-01f,3.455413282e-01f,-9.373390079e-01f,3.484186828e-01f,-9.362656474e-01f,3.512927592e-01f,-9.351835251e-01f,3.541635275e-01f,-9.340925217e-01f,3.570309579e-01f,
-9.329928160e-01f,3.598950505e-01f,-9.318842888e-01f,3.627557158e-01f,-9.307669401e-01f,3.656129837e-01f,-9.296408892e-01f,3.684668243e-01f,-9.285060763e-01f,3.713172078e-01f,-9.273625016e-01f,3.741640747e-01f,-9.262102246e-01f,3.770074248e-01f,-9.250492454e-01f,3.798471987e-01f,
-9.238795042e-01f,3.826834261e-01f,-9.227011204e-01f,3.855160475e-01f,-9.215140343e-01f,3.883450329e-01f,-9.203183055e-01f,3.911703825e-01f,-9.191138744e-01f,3.939920366e-01f,-9.179008007e-01f,3.968099952e-01f,-9.166790843e-01f,3.996241987e-01f,-9.154487252e-01f,4.024346471e-01f,
-9.142097831e-01f,4.052413106e-01f,-9.129621983e-01f,4.080441594e-01f,-9.117060304e-01f,4.108431637e-01f,-9.104412794e-01f,4.136383235e-01f,-9.091680050e-01f,4.164295495e-01f,-9.078860879e-01f,4.192169011e-01f,-9.065957069e-01f,4.220002592e-01f,-9.052967429e-01f,4.247796834e-01f,
-9.039893150e-01f,4.275550842e-01f,-9.026733041e-01f,4.303264916e-01f,-9.013488293e-01f,4.330938160e-01f,-9.000158906e-01f,4.358570874e-01f,-8.986744881e-01f,4.386162460e-01f,-8.973245621e-01f,4.413712621e-01f,-8.959662318e-01f,4.441221356e-01f,-8.945994973e-01f,4.468688369e-01f,
-8.932242990e-01f,4.496113360e-01f,-8.918406963e-01f,4.523495734e-01f,-8.904487491e-01f,4.550835788e-01f,-8.890483379e-01f,4.578132927e-01f,-8.876396418e-01f,4.605387151e-01f,-8.862225413e-01f,4.632597864e-01f,-8.847970963e-01f,4.659765065e-01f,-8.833633661e-01f,4.686888158e-01f,
-8.819212914e-01f,4.713967443e-01f,-8.804708719e-01f,4.741002023e-01f,-8.790122271e-01f,4.767992198e-01f,-8.775452971e-01f,4.794937670e-01f,-8.760700822e-01f,4.821837842e-01f,-8.745866418e-01f,4.848692417e-01f,-8.730949759e-01f,4.875501692e-01f,-8.715950847e-01f,4.902264774e-01f,
-8.700869679e-01f,4.928981960e-01f,-8.685706854e-01f,4.955652654e-01f,-8.670462370e-01f,4.982276559e-01f,-8.655136228e-01f,5.008853674e-01f,-8.639728427e-01f,5.035383701e-01f,-8.624239564e-01f,5.061866641e-01f,-8.608669639e-01f,5.088301301e-01f,-8.593018055e-01f,5.114688277e-01f,
-8.577286005e-01f,5.141027570e-01f,-8.561473489e-01f,5.167317986e-01f,-8.545579910e-01f,5.193560123e-01f,-8.529605865e-01f,5.219752789e-01f,-8.513551950e-01f,5.245896578e-01f,-8.497417569e-01f,5.271991491e-01f,-8.481203318e-01f,5.298036337e-01f,-8.464909196e-01f,5.324031115e-01f,
-8.448535800e-01f,5.349976420e-01f,-8.432082534e-01f,5.375870466e-01f,-8.415549994e-01f,5.401714444e-01f,-8.398938179e-01f,5.427507758e-01f,-8.382247090e-01f,5.453249812e-01f,-8.365477324e-01f,5.478940606e-01f,-8.348628879e-01f,5.504579544e-01f,-8.331701756e-01f,5.530167222e-01f,
-8.314695954e-01f,5.555702448e-01f,-8.297612071e-01f,5.581185222e-01f,-8.280450702e-01f,5.606615543e-01f,-8.263210654e-01f,5.631993413e-01f,-8.245893121e-01f,5.657318234e-01f,-8.228498101e-01f,5.682589412e-01f,-8.211025000e-01f,5.707807541e-01f,-8.193475008e-01f,5.732971430e-01f,
-8.175848126e-01f,5.758081675e-01f,-8.158144355e-01f,5.783137679e-01f,-8.140363097e-01f,5.808139443e-01f,-8.122506142e-01f,5.833086371e-01f,-8.104571700e-01f,5.857978463e-01f,-8.086561561e-01f,5.882815719e-01f,-8.068475723e-01f,5.907596946e-01f,-8.050313592e-01f,5.932322741e-01f,
-8.032075167e-01f,5.956993103e-01f,-8.013761640e-01f,5.981606841e-01f,-7.995372415e-01f,6.006164551e-01f,-7.976908684e-01f,6.030666232e-01f,-7.958369255e-01f,6.055110693e-01f,-7.939754725e-01f,6.079497933e-01f,-7.921065688e-01f,6.103827953e-01f,-7.902302146e-01f,6.128100753e-01f,
-7.883464098e-01f,6.152315736e-01f,-7.864552140e-01f,6.176472902e-01f,-7.845565677e-01f,6.200572252e-01f,-7.826505899e-01f,6.224612594e-01f,-7.807372212e-01f,6.248595119e-01f,-7.788165212e-01f,6.272518039e-01f,-7.768884897e-01f,6.296382546e-01f,-7.749531269e-01f,6.320187449e-01f,
-7.730104327e-01f,6.343932748e-01f,-7.710605264e-01f,6.367618442e-01f,-7.691033483e-01f,6.391244531e-01f,-7.671388984e-01f,6.414810419e-01f,-7.651672363e-01f,6.438315511e-01f,-7.631884217e-01f,6.461760402e-01f,-7.612023950e-01f,6.485143900e-01f,-7.592092156e-01f,6.508466601e-01f,
-7.572088242e-01f,6.531728506e-01f,-7.552013993e-01f,6.554928422e-01f,-7.531868219e-01f,6.578066945e-01f,-7.511651516e-01f,6.601143479e-01f,-7.491363883e-01f,6.624158025e-01f,-7.471005917e-01f,6.647109985e-01f,-7.450577617e-01f,6.669999361e-01f,-7.430079579e-01f,6.692826152e-01f,
-7.409511209e-01f,6.715589762e-01f,-7.388873100e-01f,6.738290191e-01f,-7.368165851e-01f,6.760926843e-01f,-7.347388864e-01f,6.783500314e-01f,-7.326542735e-01f,6.806010008e-01f,-7.305627465e-01f,6.828455329e-01f,-7.284643650e-01f,6.850836873e-01f,-7.263591290e-01f,6.873153448e-01f,
-7.242470980e-01f,6.895405650e-01f,-7.221282125e-01f,6.917592287e-01f,-7.200025320e-01f,6.939714551e-01f,-7.178700566e-01f,6.961771250e-01f,-7.157308459e-01f,6.983762383e-01f,-7.135848403e-01f,7.005687952e-01f,-7.114322186e-01f,7.027547359e-01f,-7.092728019e-01f,7.049340606e-01f,
-7.071067691e-01f,7.071067691e-01f,-7.049340606e-01f,7.092728019e-01f,-7.027547359e-01f,7.114322186e-01f,-7.005687952e-01f,7.135848403e-01f,-6.983762383e-01f,7.157308459e-01f,-6.961771250e-01f,7.178700566e-01f,-6.939714551e-01f,7.200025320e-01f,-6.917592287e-01f,7.221282125e-01f,
-6.895405650e-01f,7.242470980e-01f,-6.873153448e-01f,7.263591290e-01f,-6.850836873e-01f,7.284643650e-01f,-6.828455329e-01f,7.305627465e-01f,-6.806010008e-01f,7.326542735e-01f,-6.783500314e-01f,7.347388864e-01f,-6.760926843e-01f,7.368165851e-01f,-6.738290191e-01f,7.388873100e-01f,
-6.715589762e-01f,7.409511209e-01f,-6.692826152e-01f,7.430079579e-01f,-6.669999361e-01f,7.450577617e-01f,-6.647109985e-01f,7.471005917e-01f,-6.624158025e-01f,7.491363883e-01f,-6.601143479e-01f,7.511651516e-01f,-6.578066945e-01f,7.531868219e-01f,-6.554928422e-01f,7.552013993e-01f,
-6.531728506e-01f,7.572088242e-01f,-6.508466601e-01f,7.592092156e-01f,-6.485143900e-01f,7.612023950e-01f,-6.461760402e-01f,7.631884217e-01f,-6.438315511e-01f,7.651672363e-01f,-6.414810419e-01f,7.671388984e-01f,-6.391244531e-01f,7.691033483e-01f,-6.367618442e-01f,7.710605264e-01f,
-6.343932748e-01f,7.730104327e-01f,-6.320187449e-01f,7.749531269e-01f,-6.296382546e-01f,7.768884897e-01f,-6.272518039e-01f,7.788165212e-01f,-6.248595119e-01f,7.807372212e-01f,-6.224612594e-01f,7.826505899e-01f,-6.200572252e-01f,7.845565677e-01f,-6.176472902e-01f,7.864552140e-01f,
-6.152315736e-01f,7.883464098e-01f,-6.128100753e-01f,7.902302146e-01f,-6.103827953e-01f,7.921065688e-01f,-6.079497933e-01f,7.939754725e-01f,-6.055110693e-01f,7.958369255e-01f,-6.030666232e-01f,7.976908684e-01f,-6.006164551e-01f,7.995372415e-01f,-5.981606841e-01f,8.013761640e-01f,
-5.956993103e-01f,8.032075167e-01f,-5.932322741e-01f,8.050313592e-01f,-5.907596946e-01f,8.068475723e-01f,-5.882815719e-01f,8.086561561e-01f,-5.857978463e-01f,8.104571700e-01f,-5.833086371e-01f,8.122506142e-01f,-5.808139443e-01f,8.140363097e-01f,-5.783137679e-01f,8.158144355e-01f,
-5.758081675e-01f,8.175848126e-01f,-5.732971430e-01f,8.193475008e-01f,-5.707807541e-01f,8.211025000e-01f,-5.682589412e-01f,8.228498101e-01f,-5.657318234e-01f,8.245893121e-01f,-5.631993413e-01f,8.263210654e-01f,-5.606615543e-01f,8.280450702e-01f,-5.581185222e-01f,8.297612071e-01f,
-5.555702448e-01f,8.314695954e-01f,-5.530167222e-01f,8.331701756e-01f,-5.504579544e-01f,8.348628879e-01f,-5.478940606e-01f,8.365477324e-01f,-5.453249812e-01f,8.382247090e-01f,-5.427507758e-01f,8.398938179e-01f,-5.401714444e-01f,8.415549994e-01f,-5.375870466e-01f,8.432082534e-01f,
-5.349976420e-01f,8.448535800e-01f,-5.324031115e-01f,8.464909196e-01f,-5.298036337e-01f,8.481203318e-01f,-5.271991491e-01f,8.497417569e-01f,-5.245896578e-01f,8.513551950e-01f,-5.219752789e-01f,8.529605865e-01f,-5.193560123e-01f,8.545579910e-01f,-5.167317986e-01f,8.561473489e-01f,
-5.141027570e-01f,8.577286005e-01f,-5.114688277e-01f,8.593018055e-01f,-5.088301301e-01f,8.608669639e-01f,-5.061866641e-01f,8.624239564e-01f,-5.035383701e-01f,8.639728427e-01f,-5.008853674e-01f,8.655136228e-01f,-4.982276559e-01f,8.670462370e-01f,-4.955652654e-01f,8.685706854e-01f,
-4.928981960e-01f,8.700869679e-01f,-4.902264774e-01f,8.715950847e-01f,-4.875501692e-01f,8.730949759e-01f,-4.848692417e-01f,8.745866418e-01f,-4.821837842e-01f,8.760700822e-01f,-4.794937670e-01f,8.775452971e-01f,-4.767992198e-01f,8.790122271e-01f,-4.741002023e-01f,8.804708719e-01f,
-4.713967443e-01f,8.819212914e-01f,-4.686888158e-01f,8.833633661e-01f,-4.659765065e-01f,8.847970963e-01f,-4.632597864e-01f,8.862225413e-01f,-4.605387151e-01f,8.876396418e-01f,-4.578132927e-01f,8.890483379e-01f,-4.550835788e-01f,8.904487491e-01f,-4.523495734e-01f,8.918406963e-01f,
-4.496113360e-01f,8.932242990e-01f,-4.468688369e-01f,8.945994973e-01f,-4.441221356e-01f,8.959662318e-01f,-4.413712621e-01f,8.973245621e-01f,-4.386162460e-01f,8.986744881e-01f,-4.358570874e-01f,9.000158906e-01f,-4.330938160e-01f,9.013488293e-01f,-4.303264916e-01f,9.026733041e-01f,
-4.275550842e-01f,9.039893150e-01f,-4.247796834e-01f,9.052967429e-01f,-4.220002592e-01f,9.065957069e-01f,-4.192169011e-01f,9.078860879e-01f,-4.164295495e-01f,9.091680050e-01f,-4.136383235e-01f,9.104412794e-01f,-4.108431637e-01f,9.117060304e-01f,-4.080441594e-01f,9.129621983e-01f,
-4.052413106e-01f,9.142097831e-01f,-4.024346471e-01f,9.154487252e-01f,-3.996241987e-01f,9.166790843e-01f,-3.968099952e-01f,9.179008007e-01f,-3.939920366e-01f,9.191138744e-01f,-3.911703825e-01f,9.203183055e-01f,-3.883450329e-01f,9.215140343e-01f,-3.855160475e-01f,9.227011204e-01f,
-3.826834261e-01f,9.238795042e-01f,-3.798471987e-01f,9.250492454e-01f,-3.770074248e-01f,9.262102246e-01f,-3.741640747e-01f,9.273625016e-01f,-3.713172078e-01f,9.285060763e-01f,-3.684668243e-01f,9.296408892e-01f,-3.656129837e-01f,9.307669401e-01f,-3.627557158e-01f,9.318842888e-01f,
-3.598950505e-01f,9.329928160e-01f,-3.570309579e-01f,9.340925217e-01f,-3.541635275e-01f,9.351835251e-01f,-3.512927592e-01f,9.362656474e-01f,-3.484186828e-01f,9.373390079e-01f,-3.455413282e-01f,9.384035468e-01f,-3.426607251e-01f,9.394592047e-01f,-3.397768736e-01f,9.405060410e-01f,
-3.368898630e-01f,9.415440559e-01f,-3.339996636e-01f,9.425731897e-01f,-3.311063051e-01f,9.435934424e-01f,-3.282098472e-01f,9.446048141e-01f,-3.253102899e-01f,9.456073046e-01f,-3.224076927e-01f,9.466009140e-01f,-3.195020258e-01f,9.475855827e-01f,-3.165933788e-01f,9.485613704e-01f,
-3.136817515e-01f,9.495281577e-01f,-3.107671440e-01f,9.504860640e-01f,-3.078496456e-01f,9.514350295e-01f,-3.049292266e-01f,9.523749948e-01f,-3.020059466e-01f,9.533060193e-01f,-2.990798354e-01f,9.542281032e-01f,-2.961508930e-01f,9.551411867e-01f,-2.932191491e-01f,9.560452700e-01f,
-2.902846634e-01f,9.569403529e-01f,-2.873474658e-01f,9.578264356e-01f,-2.844075263e-01f,9.587034583e-01f,-2.814649343e-01f,9.595715404e-01f,-2.785196900e-01f,9.604305029e-01f,-2.755718231e-01f,9.612804651e-01f,-2.726213634e-01f,9.621214271e-01f,-2.696683109e-01f,9.629532695e-01f,
-2.667127550e-01f,9.637760520e-01f,-2.637546659e-01f,9.645897746e-01f,-2.607941031e-01f,9.653944373e-01f,-2.578310966e-01f,9.661899805e-01f,-2.548656464e-01f,9.669764638e-01f,-2.518978119e-01f,9.677538276e-01f,-2.489276081e-01f,9.685220718e-01f,-2.459550500e-01f,9.692812562e-01f,
-2.429801822e-01f,9.700312614e-01f,-2.400030196e-01f,9.707721472e-01f,-2.370236069e-01f,9.715039134e-01f,-2.340419590e-01f,9.722265005e-01f,-2.310581058e-01f,9.729399681e-01f,-2.280720770e-01f,9.736442566e-01f,-2.250839174e-01f,9.743393660e-01f,-2.220936269e-01f,9.750253558e-01f,
-2.191012353e-01f,9.757021070e-01f,-2.161068022e-01f,9.763697386e-01f,-2.131103128e-01f,9.770281315e-01f,-2.101118416e-01f,9.776773453e-01f,-2.071113735e-01f,9.783173800e-01f,-2.041089684e-01f,9.789481759e-01f,-2.011046410e-01f,9.795697927e-01f,-1.980984062e-01f,9.801821113e-01f,
-1.950903237e-01f,9.807852507e-01f,-1.920803934e-01f,9.813792109e-01f,-1.890686601e-01f,9.819638729e-01f,-1.860551536e-01f,9.825392962e-01f,-1.830398887e-01f,9.831054807e-01f,-1.800228953e-01f,9.836624265e-01f,-1.770042181e-01f,9.842100739e-01f,-1.739838719e-01f,9.847484827e-01f,
-1.709618866e-01f,9.852776527e-01f,-1.679382920e-01f,9.857975245e-01f,-1.649131179e-01f,9.863080978e-01f,-1.618863940e-01f,9.868093729e-01f,-1.588581502e-01f,9.873014092e-01f,-1.558284014e-01f,9.877841473e-01f,-1.527971923e-01f,9.882575870e-01f,-1.497645378e-01f,9.887216687e-01f,
-1.467304677e-01f,9.891765118e-01f,-1.436950266e-01f,9.896219969e-01f,-1.406582445e-01f,9.900581837e-01f,-1.376201212e-01f,9.904850721e-01f,-1.345807016e-01f,9.909026623e-01f,-1.315400302e-01f,9.913108349e-01f,-1.284981072e-01f,9.917097688e-01f,-1.254549772e-01f,9.920992851e-01f,
-1.224106774e-01f,9.924795628e-01f,-1.193652153e-01f,9.928504229e-01f,-1.163186282e-01f,9.932119250e-01f,-1.132709533e-01f,9.935641289e-01f,-1.102222055e-01f,9.939069748e-01f,-1.071724221e-01f,9.942404628e-01f,-1.041216329e-01f,9.945645928e-01f,-1.010698602e-01f,9.948793054e-01f,
-9.801714122e-02f,9.951847196e-01f,-9.496349841e-02f,9.954807758e-01f,-9.190895408e-02f,9.957674146e-01f,-8.885355294e-02f,9.960446954e-01f,-8.579730988e-02f,9.963126183e-01f,-8.274026215e-02f,9.965711236e-01f,-7.968243957e-02f,9.968202710e-01f,-7.662386447e-02f,9.970600605e-01f,
-7.356456667e-02f,9.972904325e-01f,-7.050457597e-02f,9.975114465e-01f,-6.744392216e-02f,9.977230430e-01f,-6.438262761e-02f,9.979252815e-01f,-6.132073700e-02f,9.981181026e-01f,-5.825826526e-02f,9.983015656e-01f,-5.519524589e-02f,9.984755516e-01f,-5.213170499e-02f,9.986402392e-01f,
-4.906767607e-02f,9.987954497e-01f,-4.600318149e-02f,9.989413023e-01f,-4.293825850e-02f,9.990777373e-01f,-3.987292573e-02f,9.992047548e-01f,-3.680722415e-02f,9.993223548e-01f,-3.374117240e-02f,9.994305968e-01f,-3.067480400e-02f,9.995294213e-01f,-2.760814503e-02f,9.996188283e-01f,
-2.454122901e-02f,9.996988177e-01f,-2.147408016e-02f,9.997693896e-01f,-1.840673015e-02f,9.998306036e-01f,-1.533920597e-02f,9.998823404e-01f,-1.227153838e-02f,9.999247193e-01f,-9.203754365e-03f,9.999576211e-01f,-6.135884672e-03f,9.999811649e-01f,-3.067956772e-03f,9.999952912e-01f,
-1.836970147e-16f,1.000000000e+00f,3.067956772e-03f,9.999952912e-01f,6.135884672e-03f,9.999811649e-01f,9.203754365e-03f,9.999576211e-01f,1.227153838e-02f,9.999247193e-01f,1.533920597e-02f,9.998823404e-01f,1.840673015e-02f,9.998306036e-01f,2.147408016e-02f,9.997693896e-01f,
2.454122901e-02f,9.996988177e-01f,2.760814503e-02f,9.996188283e-01f,3.067480400e-02f,9.995294213e-01f,3.374117240e-02f,9.994305968e-01f,3.680722415e-02f,9.993223548e-01f,3.987292573e-02f,9.992047548e-01f,4.293825850e-02f,9.990777373e-01f,4.600318149e-02f,9.989413023e-01f,
4.906767607e-02f,9.987954497e-01f,5.213170499e-02f,9.986402392e-01f,5.519524589e-02f,9.984755516e-01f,5.825826526e-02f,9.983015656e-01f,6.132073700e-02f,9.981181026e-01f,6.438262761e-02f,9.979252815e-01f,6.744392216e-02f,9.977230430e-01f,7.050457597e-02f,9.975114465e-01f,
7.356456667e-02f,9.972904325e-01f,7.662386447e-02f,9.970600605e-01f,7.968243957e-02f,9.968202710e-01f,8.274026215e-02f,9.965711236e-01f,8.579730988e-02f,9.963126183e-01f,8.885355294e-02f,9.960446954e-01f,9.190895408e-02f,9.957674146e-01f,9.496349841e-02f,9.954807758e-01f,
9.801714122e-02f,9.951847196e-01f,1.010698602e-01f,9.948793054e-01f,1.041216329e-01f,9.945645928e-01f,1.071724221e-01f,9.942404628e-01f,1.102222055e-01f,9.939069748e-01f,1.132709533e-01f,9.935641289e-01f,1.163186282e-01f,9.932119250e-01f,1.193652153e-01f,9.928504229e-01f,
1.224106774e-01f,9.924795628e-01f,1.254549772e-01f,9.920992851e-01f,1.284981072e-01f,9.917097688e-01f,1.315400302e-01f,9.913108349e-01f,1.345807016e-01f,9.909026623e-01f,1.376201212e-01f,9.904850721e-01f,1.406582445e-01f,9.900581837e-01f,1.436950266e-01f,9.896219969e-01f,
1.467304677e-01f,9.891765118e-01f,1.497645378e-01f,9.887216687e-01f,1.527971923e-01f,9.882575870e-01f,1.558284014e-01f,9.877841473e-01f,1.588581502e-01f,9.873014092e-01f,1.618863940e-01f,9.868093729e-01f,1.649131179e-01f,9.863080978e-01f,1.679382920e-01f,9.857975245e-01f,
1.709618866e-01f,9.852776527e-01f,1.739838719e-01f,9.847484827e-01f,1.770042181e-01f,9.842100739e-01f,1.800228953e-01f,9.836624265e-01f,1.830398887e-01f,9.831054807e-01f,1.860551536e-01f,9.825392962e-01f,1.890686601e-01f,9.819638729e-01f,1.920803934e-01f,9.813792109e-01f,
1.950903237e-01f,9.807852507e-01f,1.980984062e-01f,9.801821113e-01f,2.011046410e-01f,9.795697927e-01f,2.041089684e-01f,9.789481759e-01f,2.071113735e-01f,9.783173800e-01f,2.101118416e-01f,9.776773453e-01f,2.131103128e-01f,9.770281315e-01f,2.161068022e-01f,9.763697386e-01f,
2.191012353e-01f,9.757021070e-01f,2.220936269e-01f,9.750253558e-01f,2.250839174e-01f,9.743393660e-01f,2.280720770e-01f,9.736442566e-01f,2.310581058e-01f,9.729399681e-01f,2.340419590e-01f,9.722265005e-01f,2.370236069e-01f,9.715039134e-01f,2.400030196e-01f,9.707721472e-01f,
2.429801822e-01f,9.700312614e-01f,2.459550500e-01f,9.692812562e-01f,2.489276081e-01f,9.685220718e-01f,2.518978119e-01f,9.677538276e-01f,2.548656464e-01f,9.669764638e-01f,2.578310966e-01f,9.661899805e-01f,2.607941031e-01f,9.653944373e-01f,2.637546659e-01f,9.645897746e-01f,
2.667127550e-01f,9.637760520e-01f,2.696683109e-01f,9.629532695e-01f,2.726213634e-01f,9.621214271e-01f,2.755718231e-01f,9.612804651e-01f,2.785196900e-01f,9.604305029e-01f,2.814649343e-01f,9.595715404e-01f,2.844075263e-01f,9.587034583e-01f,2.873474658e-01f,9.578264356e-01f,
2.902846634e-01f,9.569403529e-01f,2.932191491e-01f,9.560452700e-01f,2.961508930e-01f,9.551411867e-01f,2.990798354e-01f,9.542281032e-01f,3.020059466e-01f,9.533060193e-01f,3.049292266e-01f,9.523749948e-01f,3.078496456e-01f,9.514350295e-01f,3.107671440e-01f,9.504860640e-01f,
3.136817515e-01f,9.495281577e-01f,3.165933788e-01f,9.485613704e-01f,3.195020258e-01f,9.475855827e-01f,3.224076927e-01f,9.466009140e-01f,3.253102899e-01f,9.456073046e-01f,3.282098472e-01f,9.446048141e-01f,3.311063051e-01f,9.435934424e-01f,3.339996636e-01f,9.425731897e-01f,
3.368898630e-01f,9.415440559e-01f,3.397768736e-01f,9.405060410e-01f,3.426607251e-01f,9.394592047e-01f,3.455413282e-01f,9.384035468e-01f,3.484186828e-01f,9.373390079e-01f,3.512927592e-01f,9.362656474e-01f,3.541635275e-01f,9.351835251e-01f,3.570309579e-01f,9.340925217e-01f,
3.598950505e-01f,9.329928160e-01f,3.627557158e-01f,9.318842888e-01f,3.656129837e-01f,9.307669401e-01f,3.684668243e-01f,9.296408892e-01f,3.713172078e-01f,9.285060763e-01f,3.741640747e-01f,9.273625016e-01f,3.770074248e-01f,9.262102246e-01f,3.798471987e-01f,9.250492454e-01f,
3.826834261e-01f,9.238795042e-01f,3.855160475e-01f,9.227011204e-01f,3.883450329e-01f,9.215140343e-01f,3.911703825e-01f,9.203183055e-01f,3.939920366e-01f,9.191138744e-01f,3.968099952e-01f,9.179008007e-01f,3.996241987e-01f,9.166790843e-01f,4.024346471e-01f,9.154487252e-01f,
4.052413106e-01f,9.142097831e-01f,4.080441594e-01f,9.129621983e-01f,4.108431637e-01f,9.117060304e-01f,4.136383235e-01f,9.104412794e-01f,4.164295495e-01f,9.091680050e-01f,4.192169011e-01f,9.078860879e-01f,4.220002592e-01f,9.065957069e-01f,4.247796834e-01f,9.052967429e-01f,
4.275550842e-01f,9.039893150e-01f,4.303264916e-01f,9.026733041e-01f,4.330938160e-01f,9.013488293e-01f,4.358570874e-01f,9.000158906e-01f,4.386162460e-01f,8.986744881e-01f,4.413712621e-01f,8.973245621e-01f,4.441221356e-01f,8.959662318e-01f,4.468688369e-01f,8.945994973e-01f,
4.496113360e-01f,8.932242990e-01f,4.523495734e-01f,8.918406963e-01f,4.550835788e-01f,8.904487491e-01f,4.578132927e-01f,8.890483379e-01f,4.605387151e-01f,8.876396418e-01f,4.632597864e-01f,8.862225413e-01f,4.659765065e-01f,8.847970963e-01f,4.686888158e-01f,8.833633661e-01f,
4.713967443e-01f,8.819212914e-01f,4.741002023e-01f,8.804708719e-01f,4.767992198e-01f,8.790122271e-01f,4.794937670e-01f,8.775452971e-01f,4.821837842e-01f,8.760700822e-01f,4.848692417e-01f,8.745866418e-01f,4.875501692e-01f,8.730949759e-01f,4.902264774e-01f,8.715950847e-01f,
4.928981960e-01f,8.700869679e-01f,4.955652654e-01f,8.685706854e-01f,4.982276559e-01f,8.670462370e-01f,5.008853674e-01f,8.655136228e-01f,5.035383701e-01f,8.639728427e-01f,5.061866641e-01f,8.624239564e-01f,5.088301301e-01f,8.608669639e-01f,5.114688277e-01f,8.593018055e-01f,
5.141027570e-01f,8.577286005e-01f,5.167317986e-01f,8.561473489e-01f,5.193560123e-01f,8.545579910e-01f,5.219752789e-01f,8.529605865e-01f,5.245896578e-01f,8.513551950e-01f,5.271991491e-01f,8.497417569e-01f,5.298036337e-01f,8.481203318e-01f,5.324031115e-01f,8.464909196e-01f,
5.349976420e-01f,8.448535800e-01f,5.375870466e-01f,8.432082534e-01f,5.401714444e-01f,8.415549994e-01f,5.427507758e-01f,8.398938179e-01f,5.453249812e-01f,8.382247090e-01f,5.478940606e-01f,8.365477324e-01f,5.504579544e-01f,8.348628879e-01f,5.530167222e-01f,8.331701756e-01f,
5.555702448e-01f,8.314695954e-01f,5.581185222e-01f,8.297612071e-01f,5.606615543e-01f,8.280450702e-01f,5.631993413e-01f,8.263210654e-01f,5.657318234e-01f,8.245893121e-01f,5.682589412e-01f,8.228498101e-01f,5.707807541e-01f,8.211025000e-01f,5.732971430e-01f,8.193475008e-01f,
5.758081675e-01f,8.175848126e-01f,5.783137679e-01f,8.158144355e-01f,5.808139443e-01f,8.140363097e-01f,5.833086371e-01f,8.122506142e-01f,5.857978463e-01f,8.104571700e-01f,5.882815719e-01f,8.086561561e-01f,5.907596946e-01f,8.068475723e-01f,5.932322741e-01f,8.050313592e-01f,
5.956993103e-01f,8.032075167e-01f,5.981606841e-01f,8.013761640e-01f,6.006164551e-01f,7.995372415e-01f,6.030666232e-01f,7.976908684e-01f,6.055110693e-01f,7.958369255e-01f,6.079497933e-01f,7.939754725e-01f,6.103827953e-01f,7.921065688e-01f,6.128100753e-01f,7.902302146e-01f,
6.152315736e-01f,7.883464098e-01f,6.176472902e-01f,7.864552140e-01f,6.200572252e-01f,7.845565677e-01f,6.224612594e-01f,7.826505899e-01f,6.248595119e-01f,7.807372212e-01f,6.272518039e-01f,7.788165212e-01f,6.296382546e-01f,7.768884897e-01f,6.320187449e-01f,7.749531269e-01f,
6.343932748e-01f,7.730104327e-01f,6.367618442e-01f,7.710605264e-01f,6.391244531e-01f,7.691033483e-01f,6.414810419e-01f,7.671388984e-01f,6.438315511e-01f,7.651672363e-01f,6.461760402e-01f,7.631884217e-01f,6.485143900e-01f,7.612023950e-01f,6.508466601e-01f,7.592092156e-01f,
6.531728506e-01f,7.572088242e-01f,6.554928422e-01f,7.552013993e-01f,6.578066945e-01f,7.531868219e-01f,6.601143479e-01f,7.511651516e-01f,6.624158025e-01f,7.491363883e-01f,6.647109985e-01f,7.471005917e-01f,6.669999361e-01f,7.450577617e-01f,6.692826152e-01f,7.430079579e-01f,
6.715589762e-01f,7.409511209e-01f,6.738290191e-01f,7.388873100e-01f,6.760926843e-01f,7.368165851e-01f,6.783500314e-01f,7.347388864e-01f,6.806010008e-01f,7.326542735e-01f,6.828455329e-01f,7.305627465e-01f,6.850836873e-01f,7.284643650e-01f,6.873153448e-01f,7.263591290e-01f,
6.895405650e-01f,7.242470980e-01f,6.917592287e-01f,7.221282125e-01f,6.939714551e-01f,7.200025320e-01f,6.961771250e-01f,7.178700566e-01f,6.983762383e-01f,7.157308459e-01f,7.005687952e-01f,7.135848403e-01f,7.027547359e-01f,7.114322186e-01f,7.049340606e-01f,7.092728019e-01f,
7.071067691e-01f,7.071067691e-01f,7.092728019e-01f,7.049340606e-01f,7.114322186e-01f,7.027547359e-01f,7.135848403e-01f,7.005687952e-01f,7.157308459e-01f,6.983762383e-01f,7.178700566e-01f,6.961771250e-01f,7.200025320e-01f,6.939714551e-01f,7.221282125e-01f,6.917592287e-01f,
7.242470980e-01f,6.895405650e-01f,7.263591290e-01f,6.873153448e-01f,7.284643650e-01f,6.850836873e-01f,7.305627465e-01f,6.828455329e-01f,7.326542735e-01f,6.806010008e-01f,7.347388864e-01f,6.783500314e-01f,7.368165851e-01f,6.760926843e-01f,7.388873100e-01f,6.738290191e-01f,
7.409511209e-01f,6.715589762e-01f,7.430079579e-01f,6.692826152e-01f,7.450577617e-01f,6.669999361e-01f,7.471005917e-01f,6.647109985e-01f,7.491363883e-01f,6.624158025e-01f,7.511651516e-01f,6.601143479e-01f,7.531868219e-01f,6.578066945e-01f,7.552013993e-01f,6.554928422e-01f,
7.572088242e-01f,6.531728506e-01f,7.592092156e-01f,6.508466601e-01f,7.612023950e-01f,6.485143900e-01f,7.631884217e-01f,6.461760402e-01f,7.651672363e-01f,6.438315511e-01f,7.671388984e-01f,6.414810419e-01f,7.691033483e-01f,6.391244531e-01f,7.710605264e-01f,6.367618442e-01f,
7.730104327e-01f,6.343932748e-01f,7.749531269e-01f,6.320187449e-01f,7.768884897e-01f,6.296382546e-01f,7.788165212e-01f,6.272518039e-01f,7.807372212e-01f,6.248595119e-01f,7.826505899e-01f,6.224612594e-01f,7.845565677e-01f,6.200572252e-01f,7.864552140e-01f,6.176472902e-01f,
7.883464098e-01f,6.152315736e-01f,7.902302146e-01f,6.128100753e-01f,7.921065688e-01f,6.103827953e-01f,7.939754725e-01f,6.079497933e-01f,7.958369255e-01f,6.055110693e-01f,7.976908684e-01f,6.030666232e-01f,7.995372415e-01f,6.006164551e-01f,8.013761640e-01f,5.981606841e-01f,
8.032075167e-01f,5.956993103e-01f,8.050313592e-01f,5.932322741e-01f,8.068475723e-01f,5.907596946e-01f,8.086561561e-01f,5.882815719e-01f,8.104571700e-01f,5.857978463e-01f,8.122506142e-01f,5.833086371e-01f,8.140363097e-01f,5.808139443e-01f,8.158144355e-01f,5.783137679e-01f,
8.175848126e-01f,5.758081675e-01f,8.193475008e-01f,5.732971430e-01f,8.211025000e-01f,5.707807541e-01f,8.228498101e-01f,5.682589412e-01f,8.245893121e-01f,5.657318234e-01f,8.263210654e-01f,5.631993413e-01f,8.280450702e-01f,5.606615543e-01f,8.297612071e-01f,5.581185222e-01f,
8.314695954e-01f,5.555702448e-01f,8.331701756e-01f,5.530167222e-01f,8.348628879e-01f,5.504579544e-01f,8.365477324e-01f,5.478940606e-01f,8.382247090e-01f,5.453249812e-01f,8.398938179e-01f,5.427507758e-01f,8.415549994e-01f,5.401714444e-01f,8.432082534e-01f,5.375870466e-01f,
8.448535800e-01f,5.349976420e-01f,8.464909196e-01f,5.324031115e-01f,8.481203318e-01f,5.298036337e-01f,8.497417569e-01f,5.271991491e-01f,8.513551950e-01f,5.245896578e-01f,8.529605865e-01f,5.219752789e-01f,8.545579910e-01f,5.193560123e-01f,8.561473489e-01f,5.167317986e-01f,
8.577286005e-01f,5.141027570e-01f,8.593018055e-01f,5.114688277e-01f,8.608669639e-01f,5.088301301e-01f,8.624239564e-01f,5.061866641e-01f,8.639728427e-01f,5.035383701e-01f,8.655136228e-01f,5.008853674e-01f,8.670462370e-01f,4.982276559e-01f,8.685706854e-01f,4.955652654e-01f,
8.700869679e-01f,4.928981960e-01f,8.715950847e-01f,4.902264774e-01f,8.730949759e-01f,4.875501692e-01f,8.745866418e-01f,4.848692417e-01f,8.760700822e-01f,4.821837842e-01f,8.775452971e-01f,4.794937670e-01f,8.790122271e-01f,4.767992198e-01f,8.804708719e-01f,4.741002023e-01f,
8.819212914e-01f,4.713967443e-01f,8.833633661e-01f,4.686888158e-01f,8.847970963e-01f,4.659765065e-01f,8.862225413e-01f,4.632597864e-01f,8.876396418e-01f,4.605387151e-01f,8.890483379e-01f,4.578132927e-01f,8.904487491e-01f,4.550835788e-01f,8.918406963e-01f,4.523495734e-01f,
8.932242990e-01f,4.496113360e-01f,8.945994973e-01f,4.468688369e-01f,8.959662318e-01f,4.441221356e-01f,8.973245621e-01f,4.413712621e-01f,8.986744881e-01f,4.386162460e-01f,9.000158906e-01f,4.358570874e-01f,9.013488293e-01f,4.330938160e-01f,9.026733041e-01f,4.303264916e-01f,
9.039893150e-01f,4.275550842e-01f,9.052967429e-01f,4.247796834e-01f,9.065957069e-01f,4.220002592e-01f,9.078860879e-01f,4.192169011e-01f,9.091680050e-01f,4.164295495e-01f,9.104412794e-01f,4.136383235e-01f,9.117060304e-01f,4.108431637e-01f,9.129621983e-01f,4.080441594e-01f,
9.142097831e-01f,4.052413106e-01f,9.154487252e-01f,4.024346471e-01f,9.166790843e-01f,3.996241987e-01f,9.179008007e-01f,3.968099952e-01f,9.191138744e-01f,3.939920366e-01f,9.203183055e-01f,3.911703825e-01f,9.215140343e-01f,3.883450329e-01f,9.227011204e-01f,3.855160475e-01f,
9.238795042e-01f,3.826834261e-01f,9.250492454e-01f,3.798471987e-01f,9.262102246e-01f,3.770074248e-01f,9.273625016e-01f,3.741640747e-01f,9.285060763e-01f,3.713172078e-01f,9.296408892e-01f,3.684668243e-01f,9.307669401e-01f,3.656129837e-01f,9.318842888e-01f,3.627557158e-01f,
9.329928160e-01f,3.598950505e-01f,9.340925217e-01f,3.570309579e-01f,9.351835251e-01f,3.541635275e-01f,9.362656474e-01f,3.512927592e-01f,9.373390079e-01f,3.484186828e-01f,9.384035468e-01f,3.455413282e-01f,9.394592047e-01f,3.426607251e-01f,9.405060410e-01f,3.397768736e-01f,
9.415440559e-01f,3.368898630e-01f,9.425731897e-01f,3.339996636e-01f,9.435934424e-01f,3.311063051e-01f,9.446048141e-01f,3.282098472e-01f,9.456073046e-01f,3.253102899e-01f,9.466009140e-01f,3.224076927e-01f,9.475855827e-01f,3.195020258e-01f,9.485613704e-01f,3.165933788e-01f,
9.495281577e-01f,3.136817515e-01f,9.504860640e-01f,3.107671440e-01f,9.514350295e-01f,3.078496456e-01f,9.523749948e-01f,3.049292266e-01f,9.533060193e-01f,3.020059466e-01f,9.542281032e-01f,2.990798354e-01f,9.551411867e-01f,2.961508930e-01f,9.560452700e-01f,2.932191491e-01f,
9.569403529e-01f,2.902846634e-01f,9.578264356e-01f,2.873474658e-01f,9.587034583e-01f,2.844075263e-01f,9.595715404e-01f,2.814649343e-01f,9.604305029e-01f,2.785196900e-01f,9.612804651e-01f,2.755718231e-01f,9.621214271e-01f,2.726213634e-01f,9.629532695e-01f,2.696683109e-01f,
9.637760520e-01f,2.667127550e-01f,9.645897746e-01f,2.637546659e-01f,9.653944373e-01f,2.607941031e-01f,9.661899805e-01f,2.578310966e-01f,9.669764638e-01f,2.548656464e-01f,9.677538276e-01f,2.518978119e-01f,9.685220718e-01f,2.489276081e-01f,9.692812562e-01f,2.459550500e-01f,
9.700312614e-01f,2.429801822e-01f,9.707721472e-01f,2.400030196e-01f,9.715039134e-01f,2.370236069e-01f,9.722265005e-01f,2.340419590e-01f,9.729399681e-01f,2.310581058e-01f,9.736442566e-01f,2.280720770e-01f,9.743393660e-01f,2.250839174e-01f,9.750253558e-01f,2.220936269e-01f,
9.757021070e-01f,2.191012353e-01f,9.763697386e-01f,2.161068022e-01f,9.770281315e-01f,2.131103128e-01f,9.776773453e-01f,2.101118416e-01f,9.783173800e-01f,2.071113735e-01f,9.789481759e-01f,2.041089684e-01f,9.795697927e-01f,2.011046410e-01f,9.801821113e-01f,1.980984062e-01f,
9.807852507e-01f,1.950903237e-01f,9.813792109e-01f,1.920803934e-01f,9.819638729e-01f,1.890686601e-01f,9.825392962e-01f,1.860551536e-01f,9.831054807e-01f,1.830398887e-01f,9.836624265e-01f,1.800228953e-01f,9.842100739e-01f,1.770042181e-01f,9.847484827e-01f,1.739838719e-01f,
9.852776527e-01f,1.709618866e-01f,9.857975245e-01f,1.679382920e-01f,9.863080978e-01f,1.649131179e-01f,9.868093729e-01f,1.618863940e-01f,9.873014092e-01f,1.588581502e-01f,9.877841473e-01f,1.558284014e-01f,9.882575870e-01f,1.527971923e-01f,9.887216687e-01f,1.497645378e-01f,
9.891765118e-01f,1.467304677e-01f,9.896219969e-01f,1.436950266e-01f,9.900581837e-01f,1.406582445e-01f,9.904850721e-01f,1.376201212e-01f,9.909026623e-01f,1.345807016e-01f,9.913108349e-01f,1.315400302e-01f,9.917097688e-01f,1.284981072e-01f,9.920992851e-01f,1.254549772e-01f,
9.924795628e-01f,1.224106774e-01f,9.928504229e-01f,1.193652153e-01f,9.932119250e-01f,1.163186282e-01f,9.935641289e-01f,1.132709533e-01f,9.939069748e-01f,1.102222055e-01f,9.942404628e-01f,1.071724221e-01f,9.945645928e-01f,1.041216329e-01f,9.948793054e-01f,1.010698602e-01f,
9.951847196e-01f,9.801714122e-02f,9.954807758e-01f,9.496349841e-02f,9.957674146e-01f,9.190895408e-02f,9.960446954e-01f,8.885355294e-02f,9.963126183e-01f,8.579730988e-02f,9.965711236e-01f,8.274026215e-02f,9.968202710e-01f,7.968243957e-02f,9.970600605e-01f,7.662386447e-02f,
9.972904325e-01f,7.356456667e-02f,9.975114465e-01f,7.050457597e-02f,9.977230430e-01f,6.744392216e-02f,9.979252815e-01f,6.438262761e-02f,9.981181026e-01f,6.132073700e-02f,9.983015656e-01f,5.825826526e-02f,9.984755516e-01f,5.519524589e-02f,9.986402392e-01f,5.213170499e-02f,
9.987954497e-01f,4.906767607e-02f,9.989413023e-01f,4.600318149e-02f,9.990777373e-01f,4.293825850e-02f,9.992047548e-01f,3.987292573e-02f,9.993223548e-01f,3.680722415e-02f,9.994305968e-01f,3.374117240e-02f,9.995294213e-01f,3.067480400e-02f,9.996188283e-01f,2.760814503e-02f,
9.996988177e-01f,2.454122901e-02f,9.997693896e-01f,2.147408016e-02f,9.998306036e-01f,1.840673015e-02f,9.998823404e-01f,1.533920597e-02f,9.999247193e-01f,1.227153838e-02f,9.999576211e-01f,9.203754365e-03f,9.999811649e-01f,6.135884672e-03f,9.999952912e-01f,3.067956772e-03f
};

__device__ __forceinline__ f32x2 tw(int j) { return *(const f32x2*)(TWT + 2 * j); }
__device__ __forceinline__ f32x2 cmul(f32x2 a, f32x2 b) { return f32x2{a.x * b.x - a.y * b.y, a.x * b.y + a.y * b.x}; }
__device__ __forceinline__ f32x2 cmulc(f32x2 a, f32x2 b) { return f32x2{a.x * b.x + a.y * b.y, a.y * b.x - a.x * b.y}; }
template <int INV> __device__ __forceinline__ f32x2 cmultw(f32x2 a, f32x2 w) { return INV ? cmulc(a, w) : cmul(a, w); }
__device__ __forceinline__ float bf2f(u16 v) { return __uint_as_float(((unsigned)v) << 16); }
__device__ __forceinline__ u16 f2bf(float f) { unsigned u = __float_as_uint(f); u += 0x7FFFu + ((u >> 16) & 1u); return (u16)(u >> 16); }
__device__ __forceinline__ float rbf(float f) { return bf2f(f2bf(f)); }

template <int INV> __device__ __forceinline__ f32x2 rot(f32x2 a) { return INV ? f32x2{-a.y, a.x} : f32x2{a.y, -a.x}; }
template <int INV> __device__ __forceinline__ void fft4(f32x2& a, f32x2& b, f32x2& c, f32x2& d) {
  f32x2 s0 = a + c, d0 = a - c, s1 = b + d, d1 = rot<INV>(b - d);
  a = s0 + s1; c = s0 - s1; b = d0 + d1; d = d0 - d1;
}
template <int INV> __device__ __forceinline__ void fft8(f32x2* v) {
  fft4<INV>(v[0], v[2], v[4], v[6]);
  fft4<INV>(v[1], v[3], v[5], v[7]);
  const float h = 0.70710678118654752440f;
  f32x2 o0 = v[1], o1, o2, o3;
  if (INV) { o1 = f32x2{(v[3].x - v[3].y) * h, (v[3].x + v[3].y) * h}; o3 = f32x2{(-v[7].x - v[7].y) * h, (v[7].x - v[7].y) * h}; }
  else     { o1 = f32x2{(v[3].x + v[3].y) * h, (v[3].y - v[3].x) * h}; o3 = f32x2{(v[7].y - v[7].x) * h, (-v[7].x - v[7].y) * h}; }
  o2 = rot<INV>(v[5]);
  f32x2 e0 = v[0], e1 = v[2], e2 = v[4], e3 = v[6];
  v[0] = e0 + o0; v[4] = e0 - o0; v[1] = e1 + o1; v[5] = e1 - o1;
  v[2] = e2 + o2; v[6] = e2 - o2; v[3] = e3 + o3; v[7] = e3 - o3;
}
__device__ __forceinline__ void pow7(f32x2 w, f32x2* p) {
  p[1] = w; p[2] = cmul(w, w); p[3] = cmul(p[2], w); p[4] = cmul(p[2], p[2]); p[5] = cmul(p[4], w); p[6] = cmul(p[3], p[3]); p[7] = cmul(p[4], p[3]);
}
__device__ __forceinline__ void rot16(f32x2 w, f32x2* p) {
  const float c1 = 0.92387953251128673848f, s1 = 0.38268343236508978178f, h = 0.70710678118654752440f;
  p[0] = w; p[1] = cmul(w, f32x2{c1, -s1}); p[2] = cmul(w, f32x2{h, -h}); p[3] = cmul(w, f32x2{s1, -c1});
  p[4] = f32x2{w.y, -w.x}; p[5] = cmul(w, f32x2{-s1, -c1}); p[6] = cmul(w, f32x2{-h, -h}); p[7] = cmul(w, f32x2{-c1, -s1});
}
__device__ __forceinline__ int padi(int e) { return e + (e >> 3); }

template <int CTRL, int RM> __device__ __forceinline__ float dpp_add(float v) {
  return v + __int_as_float(__builtin_amdgcn_update_dpp(0, __float_as_int(v), CTRL, RM, 0xf, false));
}
__device__ __forceinline__ float wave_sum(float v) {
  v = dpp_add<0x111, 0xf>(v); v = dpp_add<0x112, 0xf>(v); v = dpp_add<0x114, 0xf>(v); v = dpp_add<0x118, 0xf>(v);
  v = dpp_add<0x142, 0xa>(v); v = dpp_add<0x143, 0xc>(v);
  return __int_as_float(__builtin_amdgcn_readlane(__float_as_int(v), 63));
}
__device__ __forceinline__ unsigned cvt_pk(float lo, float hi) { unsigned r; asm("v_cvt_pk_bf16_f32 %0, %1, %2" : "=v"(r) : "v"(lo), "v"(hi)); return r; }
__device__ __forceinline__ float lo_f(unsigned p) { return __uint_as_float(p << 16); }
__device__ __forceinline__ float hi_f(unsigned p) { return __uint_as_float(p & 0xffff0000u); }
__device__ __forceinline__ f32x2 unpk(unsigned p) { return f32x2{lo_f(p), hi_f(p)}; }
__device__ __forceinline__ unsigned pk2(f32x2 v) { return cvt_pk(v.x, v.y); }

__device__ __forceinline__ void ln16(const f32x2* v, unsigned* o) {
  f32x2 s2 = ((v[0] + v[1]) + (v[2] + v[3])) + ((v[4] + v[5]) + (v[6] + v[7]));
  const float mean = wave_sum(s2.x + s2.y) * (1.0f / 1024.0f);
  f32x2 q2 = {0.f, 0.f};
#pragma unroll
  for (int i = 0; i < 8; ++i) { f32x2 d = v[i] - mean; q2 += d * d; }
  const float var = wave_sum(q2.x + q2.y) * (1.0f / 1024.0f);
  const float mb = rbf(mean);
  const float sd = rbf(sqrtf(rbf(rbf(var) + 1e-5f)));
  const float rinv = 1.0f / sd;
#pragma unroll
  for (int i = 0; i < 8; ++i) o[i] = pk2(unpk(pk2(v[i] - mb)) * rinv);
}

__device__ __forceinline__ void st16(void* p, f32x4 v) { asm volatile("global_store_dwordx4 %0, %1, off sc0 sc1\n\ts_nop 1" ::"v"(p), "v"(v) : "memory"); }
__device__ __forceinline__ void st16u(void* p, u32x4 v) { asm volatile("global_store_dwordx4 %0, %1, off sc0 sc1\n\ts_nop 1" ::"v"(p), "v"(v) : "memory"); }
__device__ __forceinline__ void prefetch_range(const void* p, size_t bytes, int w, int nw) {
  const size_t per = bytes / nw;
  const char* q = (const char*)p + (size_t)w * per + threadIdx.x * 16;
  u32x4 d = {0u, 0u, 0u, 0u};
  for (size_t o = 0; o < per; o += 512 * 16) asm volatile("global_load_dwordx4 %0, %1, off" : "+v"(d) : "v"(q + o) : "memory");
  asm volatile("s_waitcnt vmcnt(0)" : "+v"(d)::"memory");
}

template <int INV> __device__ __forceinline__ void fft512_wave(f32x2* v, f32x2* buf, int t, const f32x2* t2, const f32x2* t3) {
  fft8<INV>(v);
#pragma unroll
  for (int r = 0; r < 8; ++r) buf[padi(t * 8 + r)] = v[r];
  __builtin_amdgcn_wave_barrier();
  {
    const int k = t & 7;
#pragma unroll
    for (int r = 0; r < 8; ++r) { f32x2 a = buf[padi(t + 64 * r)]; v[r] = r ? cmultw<INV>(a, t2[r]) : a; }
    fft8<INV>(v);
    __builtin_amdgcn_wave_barrier();
#pragma unroll
    for (int r = 0; r < 8; ++r) buf[padi((t >> 3) * 64 + k + 8 * r)] = v[r];
    __builtin_amdgcn_wave_barrier();
  }
#pragma unroll
  for (int r = 0; r < 8; ++r) { f32x2 a = buf[padi(t + 64 * r)]; v[r] = r ? cmultw<INV>(a, t3[r]) : a; }
  fft8<INV>(v);
}

#define NKP 257
__global__ __launch_bounds__(512) void k_rows_fwd(const u16* __restrict__ x, f32x4* __restrict__ X, const u16* __restrict__ wr, const u16* __restrict__ wi, unsigned* __restrict__ WT) {
  __shared__ f32x2 sm[8 * 576 + 64];
  const int tid = threadIdx.x, w = tid >> 6, t = tid & 63;
  if (blockIdx.x >= 256) {
    const int b = blockIdx.x - 256;
    {
      unsigned* tile = (unsigned*)sm;
      const int m0 = (b & 63) * 32, k0 = (b >> 6) * 64;
      u16 a[4], c[4];
#pragma unroll
      for (int i = 0; i < 4; ++i) { const int m = i * 8 + w, k = k0 + t; const bool ok = k < NF_; const size_t o = (size_t)(m0 + m) * NF_ + (ok ? k : 0); a[i] = wr[o]; c[i] = wi[o]; }
#pragma unroll
      for (int i = 0; i < 4; ++i) tile[(i * 8 + w) * 65 + t] = (unsigned)a[i] | ((unsigned)c[i] << 16);
      __syncthreads();
#pragma unroll
      for (int i = 0; i < 2; ++i) { const int kp = i * 16 + (tid >> 5), m = tid & 31; const int k = k0 + 2 * kp;
        if (k < NF_) *(u32x2*)(WT + ((size_t)(k >> 1) * S_ + m0 + m) * 2) = u32x2{tile[m * 65 + 2 * kp], tile[m * 65 + 2 * kp + 1]}; }
    }
    return;
  }
  const int s = blockIdx.x * 8 + w;
  const unsigned* xr = (const unsigned*)(x + (size_t)s * D_);
  unsigned xu[8];
#pragma unroll
  for (int r = 0; r < 8; ++r) xu[r] = xr[t + 64 * r];
  f32x2 t2[8], t3[8], t4[8];
  { const f32x2 wa = tw(32 * (t & 7)), wb = tw(4 * t), wc_ = tw(2 * t); pow7(wa, t2); pow7(wb, t3); rot16(wc_, t4); }
  f32x2 v[8];
#pragma unroll
  for (int r = 0; r < 8; ++r) v[r] = unpk(xu[r]);
  ln16(v, xu);
#pragma unroll
  for (int r = 0; r < 8; ++r) v[r] = unpk(xu[r]);
  f32x2* buf = sm + w * 576;
  fft512_wave<0>(v, buf, t, t2, t3);
  __builtin_amdgcn_wave_barrier();
#pragma unroll
  for (int r = 0; r < 8; ++r) buf[padi(t + 64 * r)] = v[r];
  __builtin_amdgcn_wave_barrier();
  f32x2 Xo[8]; float x512 = v[0].x - v[0].y;
#pragma unroll
  for (int r = 0; r < 8; ++r) {
    const int k = t + 64 * r;
    f32x2 a = v[r], bq = buf[padi((512 - k) & 511)];
    f32x2 b = f32x2{bq.x, -bq.y};
    f32x2 E = (a + b) * 0.5f, Dd = (a - b) * 0.5f;
    f32x2 O = f32x2{Dd.y, -Dd.x};
    Xo[r] = E + cmul(O, t4[r]);
  }
  __syncthreads();
  if (t == 0) Xo[0] = f32x2{Xo[0].x, x512};
#pragma unroll
  for (int r = 0; r < 8; ++r) sm[((t >> 1) + 32 * r) * 18 + 2 * w + (t & 1)] = Xo[r];
  __syncthreads();
  const int s0 = blockIdx.x * 8;
#pragma unroll
  for (int i = 0; i < 4; ++i) {
    const int e = tid + 512 * i;
    const int kp = e >> 3, w2 = e & 7; st16(X + (size_t)kp * S_ + s0 + w2, *(const f32x4*)(sm + kp * 18 + 2 * w2));
  }
}

#define PB(e) (2 * padi(e))
template <int INV> __device__ __forceinline__ void fft2048_wg(f32x2* v, f32x2* b0, f32x2* b1, int t, const f32x2* t2, const f32x2* t3, const f32x2* t4) {
  fft8<INV>(v);
#pragma unroll
  for (int r = 0; r < 8; ++r) b0[PB(t * 8 + r)] = v[r];
  __syncthreads();
  {
    const int k = t & 7;
#pragma unroll
    for (int r = 0; r < 8; ++r) { f32x2 a = b0[PB(t + 256 * r)]; v[r] = r ? cmultw<INV>(a, t2[r]) : a; }
    fft8<INV>(v);
#pragma unroll
    for (int r = 0; r < 8; ++r) b1[PB((t >> 3) * 64 + k + 8 * r)] = v[r];
  }
  __syncthreads();
  {
    const int k = t & 63;
#pragma unroll
    for (int r = 0; r < 8; ++r) { f32x2 a = b1[PB(t + 256 * r)]; v[r] = r ? cmultw<INV>(a, t3[r]) : a; }
    fft8<INV>(v);
#pragma unroll
    for (int r = 0; r < 8; ++r) b0[PB((t >> 6) * 512 + k + 64 * r)] = v[r];
  }
  __syncthreads();
#pragma unroll
  for (int hh = 0; hh < 2; ++hh) {
    const int j = t + 256 * hh;
    f32x2 a0 = b0[PB(j)], a1 = cmultw<INV>(b0[PB(j + 512)], t4[3 * hh]), a2 = cmultw<INV>(b0[PB(j + 1024)], t4[3 * hh + 1]), a3 = cmultw<INV>(b0[PB(j + 1536)], t4[3 * hh + 2]);
    fft4<INV>(a0, a1, a2, a3);
    v[hh] = a0; v[2 + hh] = a1; v[4 + hh] = a2; v[6 + hh] = a3;
  }
}
__global__ __launch_bounds__(512) void k_cols(f32x2* __restrict__ X, const unsigned* __restrict__ WT) {
  __shared__ f32x2 bb[2][2 * 2304];
  const int c = threadIdx.x & 1, t = threadIdx.x >> 1;
  f32x2* col = X + (size_t)blockIdx.x * S_ * 2 + threadIdx.x;
  const unsigned* wc = WT + (size_t)blockIdx.x * S_ * 2 + threadIdx.x;
  f32x2 v[8]; unsigned wv[8];
#pragma unroll
  for (int r = 0; r < 8; ++r) v[r] = col[512 * r];
  f32x2 t2[8], t3[8], t4[6];
  { const f32x2 wa = tw(32 * (t & 7)), wb = tw(4 * (t & 63)), w0 = tw(t), w1 = tw(t + 256);
    pow7(wa, t2); pow7(wb, t3);
    t4[0] = w0; t4[1] = cmul(w0, w0); t4[2] = cmul(t4[1], w0); t4[3] = w1; t4[4] = cmul(w1, w1); t4[5] = cmul(t4[4], w1); }
#pragma unroll
  for (int r = 0; r < 8; ++r) wv[r] = wc[512 * r];
  f32x2* b0 = bb[0] + c; f32x2* b1 = bb[1] + c;
  fft2048_wg<0>(v, b0, b1, t, t2, t3, t4);
  if (blockIdx.x == 0) {
#pragma unroll
    for (int r = 0; r < 8; ++r) b1[PB(t + 256 * r)] = v[r];
    __syncthreads();
    if (c == 0) {
#pragma unroll
      for (int r = 0; r < 8; ++r) {
        const int m = t + 256 * r, mn = (2048 - m) & 2047;
        const f32x2 cm = v[r], cq = b1[PB(mn)], cn = f32x2{cq.x, -cq.y};
        const f32x2 F0 = (cm + cn) * 0.5f, D5 = (cm - cn) * 0.5f, F5 = f32x2{D5.y, -D5.x};
        const f32x2 w0a = unpk(wv[r]), w0b = unpk(WT[(size_t)mn * 2]), w5a = unpk(WT[((size_t)256 * S_ + m) * 2]), w5b = unpk(WT[((size_t)256 * S_ + mn) * 2]);
        const f32x2 W0s = f32x2{w0a.x + w0b.x, w0a.y - w0b.y} * 0.5f, W5s = f32x2{w5a.x + w5b.x, w5a.y - w5b.y} * 0.5f;
        const f32x2 g0 = cmul(F0, W0s), g5 = cmul(F5, W5s);
        v[r] = f32x2{g0.x - g5.y, g0.y + g5.x};
      }
    } else {
#pragma unroll
      for (int r = 0; r < 8; ++r) v[r] = cmul(v[r], unpk(wv[r]));
    }
  } else {
#pragma unroll
    for (int r = 0; r < 8; ++r) v[r] = cmul(v[r], unpk(wv[r]));
  }
  __syncthreads();
  fft2048_wg<1>(v, b0, b1, t, t2, t3, t4);
  f32x2* colp = X + (size_t)blockIdx.x * S_ * 2 + 2 * t;
#pragma unroll
  for (int q = 0; q < 4; ++q) {
    const f32x2 snd = c ? v[2 * q] : v[2 * q + 1];
    f32x2 rcv; rcv.x = __int_as_float(__builtin_amdgcn_mov_dpp(__float_as_int(snd.x), 0xB1, 0xf, 0xf, true)); rcv.y = __int_as_float(__builtin_amdgcn_mov_dpp(__float_as_int(snd.y), 0xB1, 0xf, 0xf, true));
    const f32x4 o = c ? f32x4{rcv.x, rcv.y, v[2 * q + 1].x, v[2 * q + 1].y} : f32x4{v[2 * q].x, v[2 * q].y, rcv.x, rcv.y};
    st16(colp + 512 * (2 * q + c), o);
  }
}

__global__ __launch_bounds__(512) void k_rows_inv(const f32x4* __restrict__ Y, const u16* __restrict__ x, u16* __restrict__ h, u16* __restrict__ hn) {
  __shared__ f32x2 sm[8 * 576 + 64];
  const int tid = threadIdx.x, w = tid >> 6, t = tid & 63;
  const int s0 = blockIdx.x * 8, s = s0 + w;
  f32x4 ld[4];
#pragma unroll
  for (int i = 0; i < 4; ++i) { const int e = tid + 512 * i; ld[i] = Y[(size_t)(e >> 3) * S_ + s0 + (e & 7)]; }
  const unsigned* xr = (const unsigned*)(x + (size_t)s * D_);
  unsigned xu[8];
#pragma unroll
  for (int r = 0; r < 8; ++r) xu[r] = xr[t + 64 * r];
  f32x2 t2[8], t3[8], t4[8];
  { const f32x2 wa = tw(32 * (t & 7)), wb = tw(4 * t), wc_ = tw(2 * t); pow7(wa, t2); pow7(wb, t3); rot16(wc_, t4); }
#pragma unroll
  for (int i = 0; i < 4; ++i) { const int e = tid + 512 * i; *(f32x4*)(sm + (e >> 3) * 18 + 2 * (e & 7)) = ld[i]; }
  __syncthreads();
  f32x2 v[8];
#pragma unroll
  for (int r = 0; r < 8; ++r) {
    const int k = t + 64 * r, km = (512 - k) & 511;
    f32x2 a = sm[(k >> 1) * 18 + 2 * w + (k & 1)], bq = sm[(km >> 1) * 18 + 2 * w + (km & 1)];
    f32x2 b = f32x2{bq.x, -bq.y};
    if (k == 0) { b = f32x2{a.y, 0.f}; a.y = 0.f; }
    f32x2 E = a + b, O = cmulc(a - b, t4[r]);
    v[r] = f32x2{E.x - O.y, E.y + O.x};
  }
  __syncthreads();
  fft512_wave<1>(v, sm + w * 576, t, t2, t3);
  const float sc = 1.0f / (2048.0f * 1024.0f);
  unsigned* lb = (unsigned*)(sm + w * 576);
  __builtin_amdgcn_wave_barrier();
#pragma unroll
  for (int r = 0; r < 8; ++r) {
    const unsigned hp = pk2(unpk(xu[r]) + unpk(pk2(v[r] * sc)));
    lb[t + 64 * r] = hp;
    v[r] = unpk(hp);
  }
  ln16(v, xu);
#pragma unroll
  for (int r = 0; r < 8; ++r) lb[512 + t + 64 * r] = xu[r];
  __builtin_amdgcn_wave_barrier();
#pragma unroll
  for (int i = 0; i < 2; ++i) {
    st16u(h + (size_t)s * D_ + (t + 64 * i) * 8, *(const u32x4*)(lb + (t + 64 * i) * 4));
    st16u(hn + (size_t)s * D_ + (t + 64 * i) * 8, *(const u32x4*)(lb + 512 + (t + 64 * i) * 4));
  }
}

typedef short s16x4 __attribute__((ext_vector_type(4)));
#define LAS __attribute__((address_space(3)))
#define WAIT_V(n) asm volatile("s_waitcnt vmcnt(" #n ")" ::: "memory")
#define WAIT_VN(n) asm volatile("s_waitcnt vmcnt(%0)" ::"n"(n) : "memory")
#define WAIT_L(n) asm volatile("s_waitcnt lgkmcnt(" #n ")" ::: "memory")
#define BAR() __builtin_amdgcn_s_barrier()
#define SCHED() __builtin_amdgcn_sched_barrier(0)
template <int OFF> __device__ __forceinline__ s16x4 tr_rd(unsigned addr) { s16x4 d; asm volatile("ds_read_b64_tr_b16 %0, %1 offset:%2" : "=v"(d) : "v"(addr), "n"(OFF) : "memory"); return d; }
template <int OFF, int D2> __device__ __forceinline__ bf16x8 trfrag(unsigned addr) { s16x4 a = tr_rd<OFF>(addr), b = tr_rd<OFF + D2>(addr); return bf16x8{a[0], a[1], a[2], a[3], b[0], b[1], b[2], b[3]}; }
template <int OFF> __device__ __forceinline__ bf16x8 rd128(unsigned addr) { bf16x8 d; asm volatile("ds_read_b128 %0, %1 offset:%2" : "=v"(d) : "v"(addr), "n"(OFF) : "memory"); return d; }
__device__ __forceinline__ int lds_byte(int r, int c) { const int st = (r >> 4) * 2 + (c >> 5), rr = r & 15, cc = c & 31, ob = rr * 64 + cc * 2; return st * 1024 + (ob ^ (((ob >> 9) & 1) << 5)); }
__device__ __forceinline__ void stage_rc(int b, int& R, int& C) { const int st = b / 1024, sb = b % 1024, swz = sb ^ (((sb >> 9) & 1) << 5); R = (st >> 1) * 16 + swz / 64; C = (st & 1) * 32 + (swz % 64) / 2; }
__device__ __forceinline__ f32x2 gelu_tanh2(f32x2 g) {
  const f32x2 t = g * g * 0.044715f + 1.0f;
  const f32x2 a = (g * (-2.0f * 0.7978845608028654f * 1.4426950408889634f)) * t;
  f32x2 d; d.x = __builtin_amdgcn_exp2f(a.x); d.y = __builtin_amdgcn_exp2f(a.y);
  d = d + 1.0f;
  f32x2 r; r.x = __builtin_amdgcn_rcpf(d.x); r.y = __builtin_amdgcn_rcpf(d.y);
  return g * r;
}
constexpr int HTB = 16384;
__global__ __launch_bounds__(512, 2) void gemm1(const u16* __restrict__ A, const u16* __restrict__ Bg, const u16* __restrict__ Bu, u16* __restrict__ out, const u16* __restrict__ pf) {
  constexpr int K = D_, N = DF_;
  if (blockIdx.x >= 224) { prefetch_range(pf, (size_t)D_ * DF_ * 2, blockIdx.x - 224, 32); return; }
  extern __shared__ __attribute__((aligned(16))) char smem[];
  LAS char* lds = (LAS char*)smem;
  const unsigned ldsb = (unsigned)(size_t)lds;
  const int tid = threadIdx.x, wid = __builtin_amdgcn_readfirstlane(tid >> 6), lane = tid & 63, wr = wid >> 2, wc = wid & 3, fr = lane & 15, fq = lane >> 4;
  constexpr int nt = K / 64;
  const int xcd = blockIdx.x & 7, idx = blockIdx.x >> 3;
  const int bm = (xcd & 1) * 4 + (idx & 3), bn = (xcd >> 1) * 7 + (idx >> 2);
  unsigned voffA[2], voffB[2];
#pragma unroll
  for (int i = 0; i < 2; ++i) { int R, C; stage_rc(tid * 16 + i * 8192, R, C); voffA[i] = (unsigned)(R * K + C) * 2u;
    const int row = i * 32 + wid * 4 + (lane >> 4); const int g = (row & 3) | (((row >> 3) & 1) << 2); voffB[i] = (unsigned)(row * N + (((lane & 15) ^ (g << 1)) * 8)) * 2u; }
  constexpr size_t kstepA = 128, kstepB = (size_t)64 * N * 2, hstepA = (size_t)128 * K * 2;
  const unsigned ldsw = (unsigned)wid * 1024u;
  const int aoff = lds_byte(wr * 64 + fr, fq * 8);
  const int q = fr >> 2, gq = q | ((fq & 1) << 2);
  const unsigned boff0 = ldsb + 4 * HTB + (8 * fq + q) * 256 + (((wc * 2 + 0) ^ gq) * 32) + 8 * (fr & 3), boff1 = ldsb + 4 * HTB + (8 * fq + q) * 256 + (((wc * 2 + 1) ^ gq) * 32) + 8 * (fr & 3);
#define SA(b, h) (((b) * 2 + (h)) * HTB)
#define SB(b, h) ((4 + (b) * 2 + (h)) * HTB)
#define STAGE(bufoff, gbase, voff) do { _Pragma("unroll") for (int _i = 0; _i < 2; ++_i) \
        __builtin_amdgcn_global_load_lds((const unsigned*)((const char*)(gbase) + (voff)[_i]), (LAS unsigned*)(lds + (bufoff) + ldsw + _i * 8192), 16, 0, 0); } while (0)
#define LDA(dst, b, h) do { _Pragma("unroll") for (int m = 0; m < 4; ++m) _Pragma("unroll") for (int k = 0; k < 2; ++k) dst[m][k] = *(const LAS bf16x8*)(lds + SA(b, h) + aoff + m * 2048 + k * 1024); } while (0)
#define LDB(dst, b, h) do { dst[0][0] = trfrag<((b) * 2 + (h)) * HTB, 1024>(boff0); dst[0][1] = trfrag<((b) * 2 + (h)) * HTB + 8192, 1024>(boff0); dst[1][0] = trfrag<((b) * 2 + (h)) * HTB, 1024>(boff1); dst[1][1] = trfrag<((b) * 2 + (h)) * HTB + 8192, 1024>(boff1); } while (0)
#define MMA(ai, bj, At, Bt) do { SCHED(); __builtin_amdgcn_s_setprio(1); _Pragma("unroll") for (int m = 0; m < 4; ++m) _Pragma("unroll") for (int n = 0; n < 2; ++n) _Pragma("unroll") for (int k = 0; k < 2; ++k) \
        acc[ai][bj][m][n] = __builtin_amdgcn_mfma_f32_16x16x32_bf16(Bt[n][k], At[m][k], acc[ai][bj][m][n], 0, 0, 0); __builtin_amdgcn_s_setprio(0); } while (0)
  f32x4 acc[2][2][4][2];
#pragma unroll
  for (int a = 0; a < 2; ++a)
#pragma unroll
    for (int b = 0; b < 2; ++b)
#pragma unroll
      for (int m = 0; m < 4; ++m)
#pragma unroll
        for (int n = 0; n < 2; ++n) acc[a][b][m][n] = f32x4{0.f, 0.f, 0.f, 0.f};
  bf16x8 At[4][2], B0[2][2], B1[2][2];
  const char* cA = (const char*)A + (size_t)bm * 2 * hstepA;
  const char* cG = (const char*)Bg + (size_t)bn * 256; const char* cU = (const char*)Bu + (size_t)bn * 256;
  STAGE(SB(0, 0), cG, voffB); STAGE(SA(0, 0), cA, voffA); STAGE(SB(0, 1), cU, voffB); STAGE(SA(0, 1), cA + hstepA, voffA);
  if (wr == 1) BAR();
  WAIT_V(4); BAR();
  STAGE(SB(1, 0), cG + kstepB, voffB); STAGE(SA(1, 0), cA + kstepA, voffA); STAGE(SB(1, 1), cU + kstepB, voffB);
  WAIT_V(6); BAR();
  for (int t = 0; t < nt; t += 2) {
    const bool last = (t == nt - 2);
    const int t2 = last ? 0 : t + 2;
    const char* a1 = cA + (size_t)(t + 1) * kstepA;
    const char* a2 = cA + (size_t)t2 * kstepA; const char* g2 = cG + (size_t)t2 * kstepB; const char* u2 = cU + (size_t)t2 * kstepB;
    const char* a3 = a2 + kstepA; const char* g3 = g2 + kstepB; const char* u3 = u2 + kstepB;
    LDB(B0, 0, 0); SCHED(); LDA(At, 0, 0); STAGE(SA(1, 1), a1 + hstepA, voffA);
    WAIT_L(8); BAR(); WAIT_L(0); MMA(0, 0, At, B0); BAR(); SCHED();
    LDB(B1, 0, 1); STAGE(SB(0, 0), g2, voffB);
    BAR(); WAIT_L(0); MMA(0, 1, At, B1); BAR();
    LDA(At, 0, 1); STAGE(SA(0, 0), a2, voffA);
    BAR(); WAIT_L(0); MMA(1, 0, At, B0); BAR(); SCHED();
    STAGE(SB(0, 1), u2, voffB);
    WAIT_V(6); BAR(); MMA(1, 1, At, B1); BAR();
    LDB(B0, 1, 0); SCHED(); LDA(At, 1, 0); STAGE(SA(0, 1), a2 + hstepA, voffA);
    WAIT_L(8); BAR(); WAIT_L(0); MMA(0, 0, At, B0); BAR(); SCHED();
    LDB(B1, 1, 1); STAGE(SB(1, 0), g3, voffB);
    BAR(); WAIT_L(0); MMA(0, 1, At, B1); BAR();
    LDA(At, 1, 1); STAGE(SA(1, 0), a3, voffA);
    BAR(); WAIT_L(0); MMA(1, 0, At, B0); BAR(); SCHED();
    STAGE(SB(1, 1), u3, voffB);
    WAIT_V(6); BAR(); MMA(1, 1, At, B1); BAR();
  }
  WAIT_V(0);
  if (wr == 0) BAR();
  BAR();
#pragma unroll
  for (int ai = 0; ai < 2; ++ai)
#pragma unroll
    for (int m = 0; m < 4; ++m)
#pragma unroll
      for (int n = 0; n < 2; ++n) {
        const f32x4 ag = acc[ai][0][m][n], au = acc[ai][1][m][n];
        const unsigned g01 = cvt_pk(ag[0], ag[1]), g23 = cvt_pk(ag[2], ag[3]), u01 = cvt_pk(au[0], au[1]), u23 = cvt_pk(au[2], au[3]);
        const unsigned e01 = pk2(gelu_tanh2(unpk(g01))), e23 = pk2(gelu_tanh2(unpk(g23)));
        u32x2 pk; pk.x = pk2(unpk(e01) * unpk(u01)); pk.y = pk2(unpk(e23) * unpk(u23));
        *(LAS u32x2*)(lds + (ai * 128 + wr * 64 + m * 16 + fr) * 272 + (wc * 32 + n * 16 + 4 * fq) * 2) = pk;
      }
  __syncthreads();
#pragma unroll
  for (int i = 0; i < 8; ++i) {
    const int row = i * 32 + (tid >> 4), c = tid & 15;
    u32x4 v = *(const LAS u32x4*)(lds + row * 272 + c * 16);
    st16u(out + (size_t)(bm * 256 + row) * N + bn * 128 + c * 8, v);
  }
}

template <int NST>
__global__ __launch_bounds__(512, 2) void gemm2(const u16* __restrict__ A, const u16* __restrict__ B, const u16* __restrict__ res, u16* __restrict__ out) {
  constexpr int K = DF_, N = D_;
  constexpr int A_BYTES = 128 * 128, ST = A_BYTES + 8192;
  extern __shared__ __attribute__((aligned(16))) char smem[];
  LAS char* lds = (LAS char*)smem;
  const unsigned ldsb = (unsigned)(size_t)lds;
  const int tid = threadIdx.x, wid = __builtin_amdgcn_readfirstlane(tid >> 6), lane = tid & 63;
  const int kh = wid >> 2, wr = (wid >> 1) & 1, wc = wid & 1, fr = lane & 15, fq = lane >> 4;
  const int xcd = blockIdx.x & 7, idx = blockIdx.x >> 3;
  const int bm = (xcd >> 1) * 4 + (idx & 3), bn = (xcd & 1) * 8 + (idx >> 2);
  const u16* Ag = A + (size_t)(bm * 128 + wid * 8 + (lane >> 3)) * K + ((lane & 7) ^ (lane >> 3)) * 8;
  const int brow = wid * 8 + (lane >> 3);
  const int gb = ((brow >> 1) & 1) | (((brow >> 3) & 1) << 1);
  const u16* Bg = B + (size_t)brow * N + bn * 64 + (((lane & 7) ^ (gb << 1)) * 8);
  constexpr int nk = K / 64;
  auto issue = [&](int kt, int buf) {
    LAS char* base = lds + buf * ST + wid * 1024;
    __builtin_amdgcn_global_load_lds((const unsigned*)(Ag + kt * 64), (LAS unsigned*)(base), 16, 0, 0);
    __builtin_amdgcn_global_load_lds((const unsigned*)(Ag + (size_t)64 * K + kt * 64), (LAS unsigned*)(base + 8192), 16, 0, 0);
    __builtin_amdgcn_global_load_lds((const unsigned*)(Bg + (size_t)kt * 64 * N), (LAS unsigned*)(base + A_BYTES), 16, 0, 0);
  };
  f32x4 acc[4][2];
#pragma unroll
  for (int m = 0; m < 4; ++m)
#pragma unroll
    for (int n = 0; n < 2; ++n) acc[m][n] = f32x4{0.f, 0.f, 0.f, 0.f};
  const int aoff = (wr * 64 + fr) * 128 + ((kh * 4 + fq) ^ (fr & 7)) * 16;
  const int q = fr >> 2, gq = (q >> 1) | ((fq & 1) << 1);
  unsigned boff[2];
#pragma unroll
  for (int n = 0; n < 2; ++n) boff[n] = ldsb + A_BYTES + (kh * 32 + 8 * fq + q) * 128 + (((wc * 4 + n * 2 + ((fr & 3) >> 1)) ^ (gq << 1)) * 16) + 8 * (fr & 1);
  bf16x8 a0[4], b0[2], a1[4], b1[2];
#define RDS(aa, bb, buf) do { const unsigned o_ = (buf) * ST; \
    _Pragma("unroll") for (int n = 0; n < 2; ++n) bb[n] = trfrag<0, 512>(boff[n] + o_); \
    aa[0] = rd128<0>(ldsb + o_ + aoff); aa[1] = rd128<2048>(ldsb + o_ + aoff); aa[2] = rd128<4096>(ldsb + o_ + aoff); aa[3] = rd128<6144>(ldsb + o_ + aoff); } while (0)
#define MMS(aa, bb) do { _Pragma("unroll") for (int m = 0; m < 4; ++m) _Pragma("unroll") for (int n = 0; n < 2; ++n) acc[m][n] = __builtin_amdgcn_mfma_f32_16x16x32_bf16(bb[n], aa[m], acc[m][n], 0, 0, 0); } while (0)
#pragma unroll
  for (int s = 0; s < NST - 1; ++s) issue(s, s);
  WAIT_VN((NST - 2) * 3); BAR();
  RDS(a0, b0, 0);
  int buf = 0;
  for (int kt = 0; kt < nk; kt += 2) {
    int b1i = buf + 1 == NST ? 0 : buf + 1, bp = buf == 0 ? NST - 1 : buf - 1;
    WAIT_VN((NST - 3) * 3); WAIT_L(0); BAR(); SCHED();
    { int nt = kt + NST - 1; nt = nt < nk ? nt : nk - 1; issue(nt, bp); }
    RDS(a1, b1, b1i); SCHED();
    MMS(a0, b0); SCHED();
    int b2i = b1i + 1 == NST ? 0 : b1i + 1;
    WAIT_VN((NST - 3) * 3); WAIT_L(0); BAR(); SCHED();
    { int nt = kt + NST; nt = nt < nk ? nt : nk - 1; issue(nt, buf); }
    RDS(a0, b0, b2i); SCHED();
    MMS(a1, b1); SCHED();
    buf = b2i;
  }
  WAIT_V(0); WAIT_L(0);
  BAR();
  LAS float* red = (LAS float*)lds;
  if (kh == 1) {
#pragma unroll
    for (int m = 0; m < 4; ++m)
#pragma unroll
      for (int n = 0; n < 2; ++n) *(LAS f32x4*)(red + (wr * 64 + m * 16 + fr) * 68 + wc * 32 + n * 16 + 4 * fq) = acc[m][n];
  }
  __syncthreads();
  if (kh == 0) {
#pragma unroll
    for (int m = 0; m < 4; ++m)
#pragma unroll
      for (int n = 0; n < 2; ++n) { LAS f32x4* p = (LAS f32x4*)(red + (wr * 64 + m * 16 + fr) * 68 + wc * 32 + n * 16 + 4 * fq); *p = *p + acc[m][n]; }
  }
  __syncthreads();
  {
    const int row = tid >> 2, c = (tid & 3) * 16;
    const size_t go = (size_t)(bm * 128 + row) * N + bn * 64 + c;
    u32x4 r0 = *(const u32x4*)(res + go), r1 = *(const u32x4*)(res + go + 8);
    const LAS float* p = red + row * 68 + c;
    u32x4 o0, o1;
#pragma unroll
    for (int j = 0; j < 4; ++j) {
      f32x4 v = *(const LAS f32x4*)(p + 4 * j);
      unsigned a = cvt_pk(v[0], v[1]), b = cvt_pk(v[2], v[3]);
      unsigned ra = j < 2 ? r0[2 * j] : r1[2 * j - 4], rb = j < 2 ? r0[2 * j + 1] : r1[2 * j - 3];
      unsigned oa = cvt_pk(lo_f(ra) + lo_f(a), hi_f(ra) + hi_f(a)), ob = cvt_pk(lo_f(rb) + lo_f(b), hi_f(rb) + hi_f(b));
      if (j < 2) { o0[2 * j] = oa; o0[2 * j + 1] = ob; } else { o1[2 * j - 4] = oa; o1[2 * j - 3] = ob; }
    }
    *(u32x4*)(out + go) = o0; *(u32x4*)(out + go + 8) = o1;
  }
}

extern "C" void kernel_launch(void* const* d_in, const int* in_sizes, int n_in, void* d_out, int out_size, void* d_ws, size_t ws_size, hipStream_t stream) {
  const u16* x = (const u16*)d_in[0];
  const u16* w_r = (const u16*)d_in[1];
  const u16* w_i = (const u16*)d_in[2];
  const u16* w_gate = (const u16*)d_in[4];
  const u16* w_up = (const u16*)d_in[5];
  const u16* w_down = (const u16*)d_in[6];
  u16* out = (u16*)d_out;
  char* ws = (char*)d_ws;
  size_t o = 0;
  f32x4* X1T = (f32x4*)(ws + o); o += (size_t)NKP * S_ * 16;
  unsigned* WT = (unsigned*)(ws + o); o += (size_t)NKP * S_ * 8;
  u16* h = (u16*)(ws + o); o += (size_t)S_ * D_ * 2;
  u16* hn = (u16*)(ws + o); o += (size_t)S_ * D_ * 2;
  u16* hm = (u16*)(ws + o); o += (size_t)S_ * DF_ * 2;

  k_rows_fwd<<<256 + 576, 512, 0, stream>>>(x, X1T, w_r, w_i, WT);
  k_cols<<<256, 512, 0, stream>>>((f32x2*)X1T, WT);
  k_rows_inv<<<256, 512, 0, stream>>>(X1T, x, h, hn);
  (void)hipFuncSetAttribute((const void*)gemm1, hipFuncAttributeMaxDynamicSharedMemorySize, 131072);
  gemm1<<<256, 512, 131072, stream>>>(hn, w_gate, w_up, hm, w_down);
  constexpr int NST = 6;
  (void)hipFuncSetAttribute((const void*)gemm2<NST>, hipFuncAttributeMaxDynamicSharedMemorySize, NST * 24576);
  gemm2<NST><<<256, 512, NST * 24576, stream>>>(hm, w_down, h, out);
}
```
